```python
import torch

def _softmax(x, axis=-1):
    return torch.softmax(x, dim=axis)
def _gelu(x):
    return torch.nn.functional.gelu(x, approximate="tanh")
def _silu(x):
    return torch.nn.functional.silu(x)
def _sigmoid(x):
    return torch.sigmoid(x)
def _layernorm(x, axis=-1, eps=1e-5):
    m = x.mean(dim=axis, keepdim=True)
    v = x.var(dim=axis, keepdim=True, unbiased=False)
    return (x - m) / torch.sqrt(v + eps)
def _scatter_add(tgt, idx, src, axis=0):
    return tgt.clone().index_add_(axis, idx, src)
def _segment_sum(src, seg, n):
    out = torch.zeros((n,) + src.shape[1:], dtype=src.dtype)
    return out.index_add_(0, seg, src)
def _pad(x, pw):
    flat = tuple(v for pair in reversed(pw) for v in pair)
    return torch.nn.functional.pad(x, flat)

kernel_name = 'interleave_layers__mha_block__mixer_channel_mlp__prompt'

def setup_inputs():

    _w = (lambda a: (lambda t: t.to(torch.bfloat16) if t.is_floating_point() else t)(torch.as_tensor(a)))
    def _A_setup():
        B, S, D, H = 1, 2048, 512, 32
        return {
            "x": _w((lambda *s: torch.randn(*s, dtype=torch.bfloat16) * 0.05)(B, S, D)),
            "wqkv": _w((lambda *s: torch.randn(*s, dtype=torch.bfloat16) * 0.05)(D, 3 * D)),
            "wo": _w((lambda *s: torch.randn(*s, dtype=torch.bfloat16) * 0.05)(D, D)),
        }
    _A_in = _A_setup()

    def _B_setup():
        B, S, D, F = 1, 2048, 512, 1792
        return {
            "x": _w((lambda *s: torch.randn(*s, dtype=torch.bfloat16) * 0.05)(B, S, D)),
            "w1": _w((lambda *s: torch.randn(*s, dtype=torch.bfloat16) * 3.2)(D, F) * (D ** -0.5)),
            "b1": _w((lambda *s: torch.randn(*s, dtype=torch.bfloat16) * 0.8)(F)),
            "w2": _w((lambda *s: torch.randn(*s, dtype=torch.bfloat16) * 0.2)(F, D) * (F ** -0.5)),
            "b2": _w((lambda *s: torch.randn(*s, dtype=torch.bfloat16) * 0.2)(D)),
        }
    _B_in = _B_setup()

    return {
        "A_x": _A_in["x"],
        "A_wqkv": _A_in["wqkv"],
        "A_wo": _A_in["wo"],
        "B_x": _B_in["x"],
        "B_w1": _B_in["w1"],
        "B_b1": _B_in["b1"],
        "B_w2": _B_in["w2"],
        "B_b2": _B_in["b2"],
    }

def reference(A_x, A_wqkv, A_wo, B_x, B_w1, B_b1, B_w2, B_b2):
    def _A_ref(x, wqkv, wo):
        B, S, D = x.shape
        H = 32
        Dh = D // H
        qkv = (lambda x, *d: x.permute(*d))((x @ wqkv).reshape(B, S, 3, H, Dh), 2, 0, 3, 1, 4)
        q, k, v = qkv[0], qkv[1], qkv[2]
        scores = torch.einsum("bhqd,bhkd->bhqk", q, k) * (Dh ** -0.5)
        p = _softmax(scores, axis=-1)
        o = torch.einsum("bhqk,bhkd->bhqd", p, v)
        return (lambda x, *d: x.permute(*d))(o, 0, 2, 1, 3).reshape(B, S, D) @ wo

    def _B_ref(x, w1, b1, w2, b2):
        y = _layernorm(x, axis=-1)
        h = _gelu(torch.einsum("bsd,df->bsf", y, w1) + b1)
        return torch.einsum("bsf,fd->bsd", h, w2) + b2

    x = A_x
    x = x + _A_ref(_layernorm(x, axis=-1), A_wqkv, A_wo)
    x = x + _B_ref(_layernorm(x, axis=-1), B_w1, B_b1, B_w2, B_b2)
    x = x + _A_ref(_layernorm(x, axis=-1), A_wqkv, A_wo)
    x = x + _B_ref(_layernorm(x, axis=-1), B_w1, B_b1, B_w2, B_b2)
    return x
```

```cpp
#include <hip/hip_runtime.h>
#include <hip/hip_bf16.h>
#include <stdint.h>

#define GAS __attribute__((address_space(1)))
#define LAS __attribute__((address_space(3)))
typedef unsigned u32;
typedef unsigned long long u64;
typedef u32 v4u __attribute__((ext_vector_type(4)));
typedef u32 v2u __attribute__((ext_vector_type(2)));
typedef float f32x8 __attribute__((ext_vector_type(8)));
typedef float f32x16 __attribute__((ext_vector_type(16)));
typedef __bf16 bf16x2 __attribute__((ext_vector_type(2)));
typedef __bf16 bf16x8 __attribute__((ext_vector_type(8)));
typedef __bf16 bf16x16 __attribute__((ext_vector_type(16)));
typedef short s4v __attribute__((ext_vector_type(4)));
typedef unsigned short ush;

namespace mk {
constexpr int DM = 512, FF = 1792;
constexpr u32 MB = 1u << 20;
constexpr u32 OFF_FLG = 4096, FLG_SLOT = 32768, OFF_CSQ = 1 * MB;
constexpr u32 OFF_WO = MB + MB / 2, OFF_W1 = 2 * MB, OFF_W2 = 4 * MB, OFF_WQKV = OFF_W2 + 1792 * 1024;
constexpr u32 OFF_L0 = 8 * MB, L_STRIDE = 20 * MB;
constexpr u32 LO_Q = 0, LO_KV = 2 * MB, LO_O = 6 * MB, LO_X2 = 8 * MB, LO_X3 = 10 * MB, LO_S2 = 12 * MB, LO_S3 = 12 * MB + MB / 2, LO_H = 13 * MB;
constexpr u32 WS_BYTES = 34 * MB;
constexpr int SL_QKV = 0, SL_O = 1, SL_X2 = 2, SL_H = 3, SL_X3 = 4;
constexpr u32 SPIN_BUDGET = 1u << 17;
constexpr float QSCALE = 0.25f * 1.4426950408889634f;
constexpr float GELU_TH = 0.46866869304341274f;
constexpr int L_ACT = 0, L_WO = 65536, L_STAT = 65536, L_SCR = 65536 + 4096;
constexpr int L_XRES = L_SCR + 3 * 24576, L_TAB = L_XRES + 8192, L_FLAG = L_TAB + 8 * 768, L_DUMMY = L_FLAG + 64, LDS_BYTES = L_DUMMY + 256;

typedef __amdgpu_buffer_rsrc_t rsrc_t;
__device__ __forceinline__ v4u ld16(rsrc_t rs, u32 voff, u32 soff) { return __builtin_amdgcn_raw_buffer_load_b128(rs, voff, soff, 16); }
__device__ __forceinline__ void st16(v4u v, rsrc_t rs, u32 voff, u32 soff) { __builtin_amdgcn_raw_buffer_store_b128(v, rs, voff + soff, 0, 16); }
__device__ __forceinline__ void st4(u32 v, rsrc_t rs, u32 voff, u32 soff) { __builtin_amdgcn_raw_buffer_store_b32(v, rs, voff, soff, 16); }
__device__ __forceinline__ u32 pk2(float a, float b) { bf16x2 r = {(__bf16)a, (__bf16)b}; return __builtin_bit_cast(u32, r); }
__device__ __forceinline__ float max_halves(float v) {
  const u32 u = __builtin_bit_cast(u32, v);
  auto r = __builtin_amdgcn_permlane32_swap(u, u, false, false);
  const u32 r0 = r[0], r1 = r[1];
  return fmaxf(__builtin_bit_cast(float, r0), __builtin_bit_cast(float, r1));
}
#define US(x) ((u32)__builtin_amdgcn_readfirstlane((int)(x)))

constexpr int TABSZ = 768;

__device__ __forceinline__ void publish(rsrc_t rs, u32 off, u64 tag, int w, int lane) {
  asm volatile("" : "+s"(tag));
  v4u t = {(u32)tag, (u32)(tag >> 32), (u32)tag, (u32)(tag >> 32)};
  u32 vo = lane * 16 + off;
  asm volatile("s_waitcnt vmcnt(0)" : "+v"(t), "+v"(vo) :: "memory");
  __syncthreads();
  if (w == 0 && lane < 8) __builtin_amdgcn_raw_buffer_store_b128(t, rs, vo, 0, 16);
}
__device__ __forceinline__ u32 flag_off(int slot, int idx) { return OFF_FLG + slot * FLG_SLOT + idx * 128; }
__device__ __forceinline__ v4u mkdesc(const void* p, u32 bytes) { return v4u{(u32)(u64)p, (u32)((u64)p >> 32) & 0xffffu, bytes, 0x00020000u}; }
__device__ __forceinline__ int qkv_tile(int w, int j) { return (w < 4 ? (w >> 1) * 16 : 32) + 2 * j + (w & 1); }
__device__ __forceinline__ int w2_pos(int p, int k) {
  if (k < 8) return 32 * (k >> 1) + 4 * p + 2 * (k & 1);
  if (p == 0 && k < 12) return (k < 10 ? 128 : 154) + 2 * (k & 1);
  const int q = p ? 6 * (p - 1) + (k - 8) : 30 + k;
  return 132 + 2 * q + (q >= 11 ? 4 : 0);
}
__device__ __forceinline__ u32 qkv_out(u32 LB, int w, int cq, int jq, int lane) {
  const int hd = 2 * (2 * jq + (w & 1)), tok = lane & 31, h = lane >> 5;
  const u32 a = LB + LO_Q + (hd * 64 + 2 * cq) * 1024 + lane * 16;
  const u32 b = LB + LO_KV + (hd * 64 + 2 * cq) * 2048 + (((tok & 19) | ((tok & 4) << 1) | ((tok & 8) >> 1)) + 32 * h) * 16;
  const u32 c = LB + LO_KV + (hd * 64 + 2 * cq + ((w >> 1) & 1)) * 2048 + 1024 + (tok >> 4) * (64 * 2048) + (h * 16 + (tok & 15)) * 16;
  return w < 2 ? a : w < 4 ? b : c;
}

__global__ void __launch_bounds__(512, 2) __attribute__((amdgpu_num_vgpr(64)))
mega(const ush* xin, const ush* wqkv, const ush* wo, const ush* w1, const ush* b1, const ush* w2, const ush* b2, ush* out, char* ws) {
  asm volatile("" :: "s"(xin), "s"(wqkv), "s"(wo), "s"(w1), "s"(b1), "s"(w2), "s"(b2), "s"(out), "s"(ws));
  extern __shared__ __attribute__((aligned(16))) char smem[];
  LAS char* L = (LAS char*)smem;
  const u32 lds0 = (u32)(size_t)L;
  const int w_ = __builtin_amdgcn_readfirstlane(threadIdx.x >> 6);
#define PHASE_VARS() \
  int lane, w = w_, b = blockIdx.x; \
  asm volatile("v_mbcnt_lo_u32_b32 %0, -1, 0\n\tv_mbcnt_hi_u32_b32 %0, -1, %0" : "=v"(lane)); asm volatile("" : "+s"(w)); asm volatile("" : "+s"(b)); \
  const int xc = b & 7, bi = b >> 3, c = xc * 4 + (bi >> 3), j = bi & 7, tok = lane & 31, h = lane >> 5; \
  const int c0 = (xc >> 1) * 8 + (bi >> 2), j0 = (xc & 1) * 4 + (bi & 3); \
  const u32 l16 = lane * 16, ltab = lds0 + L_TAB + w * TABSZ; \
  LAS float* tab = (LAS float*)(L + L_TAB + w * TABSZ); \
  (void)xc; (void)bi; (void)c; (void)j; (void)c0; (void)j0; (void)tok; (void)h; (void)l16; (void)tab; (void)ltab;
  const rsrc_t rs = __builtin_amdgcn_make_buffer_rsrc((void*)ws, 0, WS_BYTES, 0x00020000);
  const v4u desc = mkdesc(ws, WS_BYTES);
  u64 tag;
  u32 bud = SPIN_BUDGET;

  {
    PHASE_VARS();
    const v4u dq = mkdesc(wqkv, 512 * 3072), dx = mkdesc(xin, 2048 * 1024);
    const u32 dummy = US(lds0 + L_DUMMY);
    asm volatile("s_load_dwordx2 %0, %1, 0x0" : "=s"(tag) : "s"(ws) : "memory");
    if (w == 7) {
      u64 pc; u32 len;
      asm volatile("s_getpc_b64 %0\nMK_CODE_FROM:\n\ts_mov_b32 %1, MK_CODE_TO-MK_CODE_FROM" : "=s"(pc), "=s"(len));
      asm volatile("s_mov_b32 m0, %2\n\ts_nop 4\n\tbuffer_load_dword %0, %1, 0 offen lds" :: "v"((bi * 16 + (lane & 15)) * 128), "s"(mkdesc((const void*)pc, len)), "s"(dummy) : "memory");
    }
    u32 rot = US(((bi >> 2) + 2 * (bi & 3)) & 7), t0;
    const u32 vraw = (w * 8 + (lane >> 3)) * 3072 + j0 * 128 + ((lane & 7) ^ ((lane >> 2) & 4)) * 16;
    const u32 vxa = (64 * c0 + 8 * w + (lane >> 3)) * 1024 + (((lane & 7) ^ ((4 * w + (lane >> 4)) & 7)) * 16);
    const u32 mw = US(lds0 + L_SCR + w * 3072), mx = US(lds0 + L_ACT + w * 1024);
    asm volatile(
          "s_nop 4\n\ts_add_u32 %[t0], %[mw], 0\n\ts_mov_b32 m0, %[t0]\n\ts_mul_i32 %[t0], %[rot], 196608\n\t"
          "buffer_load_dwordx4 %[vraw], %[descr], %[t0] offen offset:0 lds\n\tbuffer_load_dwordx4 %[vraw], %[descr], %[t0] offen offset:1024 lds\n\tbuffer_load_dwordx4 %[vraw], %[descr], %[t0] offen offset:2048 lds\n\ts_add_u32 %[t0], %[mx], 0\n\t"
          "s_mov_b32 m0, %[t0]\n\ts_lshl_b32 %[t0], %[rot], 7\n\tbuffer_load_dwordx4 %[vxa], %[descx], %[t0] offen lds\n\ts_add_u32 %[rot], %[rot], 1\n\t"
          "s_and_b32 %[rot], %[rot], 7\n\t"
        : [rot] "+s"(rot), [t0] "=&s"(t0)
        : [vraw] "v"(vraw), [vxa] "v"(vxa), [descr] "s"(dq), [descx] "s"(dx), [mw] "s"(mw), [mx] "s"(mx)
        : "scc", "memory", "v255");
    const ush* src; int jld, np, ks0; u32 jd[4];
    if (b < 120) {
      u32 base; int ja, jb;
      if (b < 48) { src = wqkv; jld = 1536; np = b >> 1; base = OFF_WQKV; ja = jb = np & 7; }
      else if (b < 104) { src = w1; jld = FF; np = (b - 48) >> 1; base = OFF_W1; ja = (2 * np) / 7; jb = (2 * np + 1) / 7; }
      else { src = wo; jld = DM; np = (b - 104) >> 1; base = OFF_WO; ja = jb = 0; }
      ks0 = 16 * (b & 1) + 2 * w;
      jd[0] = base + (2 * np * 32 + ((ks0 - 4 * ja) & 31)) * 1024; jd[1] = base + ((2 * np + 1) * 32 + ((ks0 - 4 * jb) & 31)) * 1024;
      jd[2] = jd[0] + 1024; jd[3] = jd[1] + 1024;
    } else if (b < 184 && w < 7) {
      const int m = (b - 120) & 7; src = w2; jld = DM; np = (b - 120) >> 3; ks0 = 14 * m + 2 * w;
      jd[0] = OFF_W2 + (np * 224 + w2_pos((m - np) & 7, 2 * w)) * 1024; jd[2] = OFF_W2 + (np * 224 + w2_pos((m - np) & 7, 2 * w + 1)) * 1024;
      jd[1] = jd[0] + 1024; jd[3] = jd[2] + 1024;
    } else { src = wo; jld = DM; np = 0; ks0 = 0; jd[0] = jd[1] = jd[2] = jd[3] = WS_BYTES; }
    asm volatile("s_waitcnt lgkmcnt(0)" : "+s"(tag) :: "memory");
    tag += 1;
    {
      const int q = (lane & 15) >> 2;
      asm volatile(
          "s_nop 4\n\ts_cmp_ge_u32 %[w], 4\n\ts_cbranch_scc1 RV%=\n\ts_add_u32 %[t0], %[mw], 24576\n\t"
          "s_mov_b32 m0, %[t0]\n\ts_mul_i32 %[t0], %[rot], 196608\n\tbuffer_load_dwordx4 %[vraw], %[descr], %[t0] offen offset:0 lds\n\tbuffer_load_dwordx4 %[vraw], %[descr], %[t0] offen offset:1024 lds\n\t"
          "buffer_load_dwordx4 %[vraw], %[descr], %[t0] offen offset:2048 lds\n\ts_add_u32 %[t0], %[mx], 8192\n\ts_mov_b32 m0, %[t0]\n\ts_lshl_b32 %[t0], %[rot], 7\n\t"
          "buffer_load_dwordx4 %[vxa], %[descx], %[t0] offen lds\n\ts_add_u32 %[rot], %[rot], 1\n\ts_and_b32 %[rot], %[rot], 7\n\tv_mov_b32 v68, 0\n\t"
          "v_mov_b32 v69, 0\n\tv_mov_b32 v70, 0x3f803f80\n\tv_mov_b32 v72, 0\n\tv_mov_b32 v73, 0\n\t"
          "v_add_u32 v84, 0x8000, %[vtr]\n\tv_add_u32 v64, %[mx], %[l16]\n\tv_xor_b32 v65, 32, %[vr]\n\tv_xor_b32 v71, 64, %[vr]\n\t"
          "v_xor_b32 v85, 96, %[vr]\n\tv_and_b32 v164, 0xe0, %[l16]\n\tv_mov_b32 v161, 0x3f803f80\n\tv_cmp_eq_u32 vcc, 0, v164\n\t"
          "v_mov_b32 v168, 0\n\tv_mov_b32 v169, 0\n\tv_cndmask_b32 v160, 0, v161, vcc\n\tv_cmp_eq_u32 vcc, 32, v164\n\t"
          "v_mov_b32 v170, 0\n\tv_and_b32 v164, 16, %[l16]\n\tv_cndmask_b32 v162, 0, v161, vcc\n\tv_cmp_eq_u32 vcc, 0, v164\n\t"
          "v_mov_b32 v161, 0x4444\n\tv_mov_b32 v164, 0xeeee\n\tv_cndmask_b32 v164, v164, v161, vcc\n\tv_mov_b32 v161, v160\n\t"
          "v_mov_b32 v163, v162\n\tv_mov_b32 v171, 0\n\ts_nop 1\n\ts_waitcnt vmcnt(4)\n\t"
          "s_waitcnt lgkmcnt(0)\n\ts_barrier\n\tds_read_b64_tr_b16 v[128:129], %[vtr] offset:0\n\tds_read_b64_tr_b16 v[130:131], %[vtr] offset:512\n\t"
          "ds_read_b64_tr_b16 v[132:133], %[vtr] offset:6144\n\tds_read_b64_tr_b16 v[134:135], %[vtr] offset:6656\n\tds_read_b64_tr_b16 v[136:137], %[vtr] offset:12288\n\tds_read_b64_tr_b16 v[138:139], %[vtr] offset:12800\n\t"
          "ds_read_b64_tr_b16 v[140:141], %[vtr] offset:18432\n\tds_read_b64_tr_b16 v[142:143], %[vtr] offset:18944\n\ts_waitcnt lgkmcnt(4)\n\tds_read_b128 v[32:35], %[vr] offset:0\n\t"
          "ds_read_b128 v[36:39], %[vr] offset:4096\n\tds_read_b128 v[40:43], v65 offset:0\n\tds_read_b128 v[44:47], v65 offset:4096\n\tds_read_b128 v[48:51], v71 offset:0\n\t"
          "ds_read_b128 v[52:55], v71 offset:4096\n\tds_read_b128 v[56:59], v85 offset:0\n\tds_read_b128 v[60:63], v85 offset:4096\n\tds_read_b128 v[76:79], v64 offset:0\n\t"
          "s_add_u32 %[t0], %[mw], 49152\n\ts_mov_b32 m0, %[t0]\n\ts_mul_i32 %[t0], %[rot], 196608\n\tbuffer_load_dwordx4 %[vraw], %[descr], %[t0] offen offset:0 lds\n\t"
          "buffer_load_dwordx4 %[vraw], %[descr], %[t0] offen offset:1024 lds\n\tbuffer_load_dwordx4 %[vraw], %[descr], %[t0] offen offset:2048 lds\n\ts_add_u32 %[t0], %[mx], 16384\n\ts_mov_b32 m0, %[t0]\n\t"
          "s_lshl_b32 %[t0], %[rot], 7\n\tbuffer_load_dwordx4 %[vxa], %[descx], %[t0] offen lds\n\ts_add_u32 %[rot], %[rot], 1\n\ts_and_b32 %[rot], %[rot], 7\n\t"
          "s_waitcnt vmcnt(4)\n\ts_waitcnt lgkmcnt(0)\n\ts_barrier\n\tv_mfma_f32_32x32x16_bf16 v[0:15], v[128:131], v[32:35], 0\n\t"
          "ds_read_b64_tr_b16 v[144:145], %[vtr] offset:24576\n\tds_read_b64_tr_b16 v[146:147], %[vtr] offset:25088\n\tds_read_b64_tr_b16 v[148:149], %[vtr] offset:30720\n\tv_mfma_f32_32x32x16_bf16 v[16:31], v[128:131], v[36:39], 0\n\t"
          "ds_read_b64_tr_b16 v[150:151], %[vtr] offset:31232\n\tds_read_b64_tr_b16 v[152:153], %[vtr] offset:36864\n\tds_read_b64_tr_b16 v[154:155], %[vtr] offset:37376\n\tv_dot2c_f32_bf16 v72, v70, v76\n\t"
          "v_dot2c_f32_bf16 v73, v76, v76\n\tv_mfma_f32_32x32x16_bf16 v[0:15], v[132:135], v[40:43], v[0:15]\n\tds_read_b64_tr_b16 v[156:157], %[vtr] offset:43008\n\tds_read_b64_tr_b16 v[158:159], %[vtr] offset:43520\n\t"
          "ds_read_b128 v[96:99], %[vr] offset:8192\n\tv_mfma_f32_32x32x16_bf16 v[16:31], v[132:135], v[44:47], v[16:31]\n\ts_waitcnt lgkmcnt(8)\n\tds_read_b128 v[100:103], %[vr] offset:12288\n\t"
          "ds_read_b128 v[104:107], v65 offset:8192\n\tds_read_b128 v[108:111], v65 offset:12288\n\tv_smfmac_f32_16x16x64_bf16 v[168:171], v[160:163], v[128:135], v164\n\tv_dot2c_f32_bf16 v72, v70, v77\n\t"
          "v_dot2c_f32_bf16 v73, v77, v77\n\tv_mfma_f32_32x32x16_bf16 v[0:15], v[136:139], v[48:51], v[0:15]\n\ts_waitcnt lgkmcnt(8)\n\tds_read_b128 v[112:115], v71 offset:8192\n\t"
          "ds_read_b128 v[116:119], v71 offset:12288\n\tds_read_b128 v[120:123], v85 offset:8192\n\tv_mfma_f32_32x32x16_bf16 v[16:31], v[136:139], v[52:55], v[16:31]\n\ts_waitcnt lgkmcnt(8)\n\t"
          "ds_read_b128 v[124:127], v85 offset:12288\n\tds_read_b128 v[80:83], v64 offset:8192\n\tv_dot2c_f32_bf16 v72, v70, v78\n\tv_dot2c_f32_bf16 v73, v78, v78\n\t"
          "v_mfma_f32_32x32x16_bf16 v[0:15], v[140:143], v[56:59], v[0:15]\n\tv_mfma_f32_32x32x16_bf16 v[16:31], v[140:143], v[60:63], v[16:31]\n\ts_add_u32 %[t0], %[mw], 0\n\ts_mov_b32 m0, %[t0]\n\t"
          "s_mul_i32 %[t0], %[rot], 196608\n\tbuffer_load_dwordx4 %[vraw], %[descr], %[t0] offen offset:0 lds\n\tbuffer_load_dwordx4 %[vraw], %[descr], %[t0] offen offset:1024 lds\n\tbuffer_load_dwordx4 %[vraw], %[descr], %[t0] offen offset:2048 lds\n\t"
          "s_add_u32 %[t0], %[mx], 24576\n\ts_mov_b32 m0, %[t0]\n\ts_lshl_b32 %[t0], %[rot], 7\n\tbuffer_load_dwordx4 %[vxa], %[descx], %[t0] offen lds\n\t"
          "s_add_u32 %[rot], %[rot], 1\n\ts_and_b32 %[rot], %[rot], 7\n\tv_smfmac_f32_16x16x64_bf16 v[168:171], v[160:163], v[136:143], v164\n\tv_dot2c_f32_bf16 v72, v70, v79\n\t"
          "v_dot2c_f32_bf16 v73, v79, v79\n\ts_waitcnt vmcnt(4)\n\ts_waitcnt lgkmcnt(0)\n\ts_barrier\n\t"
          "v_mfma_f32_32x32x16_bf16 v[0:15], v[144:147], v[96:99], v[0:15]\n\tds_read_b64_tr_b16 v[128:129], v84 offset:16384\n\tds_read_b64_tr_b16 v[130:131], v84 offset:16896\n\tds_read_b64_tr_b16 v[132:133], v84 offset:22528\n\t"
          "v_mfma_f32_32x32x16_bf16 v[16:31], v[144:147], v[100:103], v[16:31]\n\tds_read_b64_tr_b16 v[134:135], v84 offset:23040\n\tds_read_b64_tr_b16 v[136:137], v84 offset:28672\n\tds_read_b64_tr_b16 v[138:139], v84 offset:29184\n\t"
          "v_dot2c_f32_bf16 v72, v70, v80\n\tv_dot2c_f32_bf16 v73, v80, v80\n\tv_mfma_f32_32x32x16_bf16 v[0:15], v[148:151], v[104:107], v[0:15]\n\tds_read_b64_tr_b16 v[140:141], v84 offset:34816\n\t"
          "ds_read_b64_tr_b16 v[142:143], v84 offset:35328\n\tds_read_b128 v[32:35], %[vr] offset:16384\n\tv_mfma_f32_32x32x16_bf16 v[16:31], v[148:151], v[108:111], v[16:31]\n\ts_waitcnt lgkmcnt(8)\n\t"
          "ds_read_b128 v[36:39], %[vr] offset:20480\n\tds_read_b128 v[40:43], v65 offset:16384\n\tds_read_b128 v[44:47], v65 offset:20480\n\tv_smfmac_f32_16x16x64_bf16 v[168:171], v[160:163], v[144:151], v164\n\t"
          "v_dot2c_f32_bf16 v72, v70, v81\n\tv_dot2c_f32_bf16 v73, v81, v81\n\tv_mfma_f32_32x32x16_bf16 v[0:15], v[152:155], v[112:115], v[0:15]\n\ts_waitcnt lgkmcnt(8)\n\t"
          "ds_read_b128 v[48:51], v71 offset:16384\n\tds_read_b128 v[52:55], v71 offset:20480\n\tds_read_b128 v[56:59], v85 offset:16384\n\tv_mfma_f32_32x32x16_bf16 v[16:31], v[152:155], v[116:119], v[16:31]\n\t"
          "s_waitcnt lgkmcnt(8)\n\tds_read_b128 v[60:63], v85 offset:20480\n\tds_read_b128 v[76:79], v64 offset:16384\n\tv_dot2c_f32_bf16 v72, v70, v82\n\t"
          "v_dot2c_f32_bf16 v73, v82, v82\n\tv_mfma_f32_32x32x16_bf16 v[0:15], v[156:159], v[120:123], v[0:15]\n\tv_mfma_f32_32x32x16_bf16 v[16:31], v[156:159], v[124:127], v[16:31]\n\ts_add_u32 %[t0], %[mw], 24576\n\t"
          "s_mov_b32 m0, %[t0]\n\ts_mul_i32 %[t0], %[rot], 196608\n\tbuffer_load_dwordx4 %[vraw], %[descr], %[t0] offen offset:0 lds\n\tbuffer_load_dwordx4 %[vraw], %[descr], %[t0] offen offset:1024 lds\n\t"
          "buffer_load_dwordx4 %[vraw], %[descr], %[t0] offen offset:2048 lds\n\ts_add_u32 %[t0], %[mx], 32768\n\ts_mov_b32 m0, %[t0]\n\ts_lshl_b32 %[t0], %[rot], 7\n\t"
          "buffer_load_dwordx4 %[vxa], %[descx], %[t0] offen lds\n\ts_add_u32 %[rot], %[rot], 1\n\ts_and_b32 %[rot], %[rot], 7\n\tv_smfmac_f32_16x16x64_bf16 v[168:171], v[160:163], v[152:159], v164\n\t"
          "v_dot2c_f32_bf16 v72, v70, v83\n\tv_dot2c_f32_bf16 v73, v83, v83\n\ts_waitcnt vmcnt(4)\n\ts_waitcnt lgkmcnt(0)\n\t"
          "s_barrier\n\tv_mfma_f32_32x32x16_bf16 v[0:15], v[128:131], v[32:35], v[0:15]\n\tds_read_b64_tr_b16 v[144:145], %[vtr] offset:0\n\tds_read_b64_tr_b16 v[146:147], %[vtr] offset:512\n\t"
          "ds_read_b64_tr_b16 v[148:149], %[vtr] offset:6144\n\tv_mfma_f32_32x32x16_bf16 v[16:31], v[128:131], v[36:39], v[16:31]\n\tds_read_b64_tr_b16 v[150:151], %[vtr] offset:6656\n\tds_read_b64_tr_b16 v[152:153], %[vtr] offset:12288\n\t"
          "ds_read_b64_tr_b16 v[154:155], %[vtr] offset:12800\n\tv_dot2c_f32_bf16 v72, v70, v76\n\tv_dot2c_f32_bf16 v73, v76, v76\n\tv_mfma_f32_32x32x16_bf16 v[0:15], v[132:135], v[40:43], v[0:15]\n\t"
          "ds_read_b64_tr_b16 v[156:157], %[vtr] offset:18432\n\tds_read_b64_tr_b16 v[158:159], %[vtr] offset:18944\n\tds_read_b128 v[96:99], %[vr] offset:24576\n\tv_mfma_f32_32x32x16_bf16 v[16:31], v[132:135], v[44:47], v[16:31]\n\t"
          "s_waitcnt lgkmcnt(8)\n\tds_read_b128 v[100:103], %[vr] offset:28672\n\tds_read_b128 v[104:107], v65 offset:24576\n\tds_read_b128 v[108:111], v65 offset:28672\n\t"
          "v_smfmac_f32_16x16x64_bf16 v[168:171], v[160:163], v[128:135], v164\n\tv_dot2c_f32_bf16 v72, v70, v77\n\tv_dot2c_f32_bf16 v73, v77, v77\n\tv_mfma_f32_32x32x16_bf16 v[0:15], v[136:139], v[48:51], v[0:15]\n\t"
          "s_waitcnt lgkmcnt(8)\n\tds_read_b128 v[112:115], v71 offset:24576\n\tds_read_b128 v[116:119], v71 offset:28672\n\tds_read_b128 v[120:123], v85 offset:24576\n\t"
          "v_mfma_f32_32x32x16_bf16 v[16:31], v[136:139], v[52:55], v[16:31]\n\ts_waitcnt lgkmcnt(8)\n\tds_read_b128 v[124:127], v85 offset:28672\n\tds_read_b128 v[80:83], v64 offset:24576\n\t"
          "v_dot2c_f32_bf16 v72, v70, v78\n\tv_dot2c_f32_bf16 v73, v78, v78\n\tv_mfma_f32_32x32x16_bf16 v[0:15], v[140:143], v[56:59], v[0:15]\n\tv_mfma_f32_32x32x16_bf16 v[16:31], v[140:143], v[60:63], v[16:31]\n\t"
          "s_add_u32 %[t0], %[mw], 49152\n\ts_mov_b32 m0, %[t0]\n\ts_mul_i32 %[t0], %[rot], 196608\n\tbuffer_load_dwordx4 %[vraw], %[descr], %[t0] offen offset:0 lds\n\t"
          "buffer_load_dwordx4 %[vraw], %[descr], %[t0] offen offset:1024 lds\n\tbuffer_load_dwordx4 %[vraw], %[descr], %[t0] offen offset:2048 lds\n\ts_add_u32 %[t0], %[mx], 40960\n\ts_mov_b32 m0, %[t0]\n\t"
          "s_lshl_b32 %[t0], %[rot], 7\n\tbuffer_load_dwordx4 %[vxa], %[descx], %[t0] offen lds\n\ts_add_u32 %[rot], %[rot], 1\n\ts_and_b32 %[rot], %[rot], 7\n\t"
          "v_smfmac_f32_16x16x64_bf16 v[168:171], v[160:163], v[136:143], v164\n\tv_dot2c_f32_bf16 v72, v70, v79\n\tv_dot2c_f32_bf16 v73, v79, v79\n\ts_waitcnt vmcnt(4)\n\t"
          "s_waitcnt lgkmcnt(0)\n\ts_barrier\n\tv_mfma_f32_32x32x16_bf16 v[0:15], v[144:147], v[96:99], v[0:15]\n\tds_read_b64_tr_b16 v[128:129], %[vtr] offset:24576\n\t"
          "ds_read_b64_tr_b16 v[130:131], %[vtr] offset:25088\n\tds_read_b64_tr_b16 v[132:133], %[vtr] offset:30720\n\tv_mfma_f32_32x32x16_bf16 v[16:31], v[144:147], v[100:103], v[16:31]\n\tds_read_b64_tr_b16 v[134:135], %[vtr] offset:31232\n\t"
          "ds_read_b64_tr_b16 v[136:137], %[vtr] offset:36864\n\tds_read_b64_tr_b16 v[138:139], %[vtr] offset:37376\n\tv_dot2c_f32_bf16 v72, v70, v80\n\tv_dot2c_f32_bf16 v73, v80, v80\n\t"
          "v_mfma_f32_32x32x16_bf16 v[0:15], v[148:151], v[104:107], v[0:15]\n\tds_read_b64_tr_b16 v[140:141], %[vtr] offset:43008\n\tds_read_b64_tr_b16 v[142:143], %[vtr] offset:43520\n\tds_read_b128 v[32:35], %[vr] offset:32768\n\t"
          "v_mfma_f32_32x32x16_bf16 v[16:31], v[148:151], v[108:111], v[16:31]\n\ts_waitcnt lgkmcnt(8)\n\tds_read_b128 v[36:39], %[vr] offset:36864\n\tds_read_b128 v[40:43], v65 offset:32768\n\t"
          "ds_read_b128 v[44:47], v65 offset:36864\n\tv_smfmac_f32_16x16x64_bf16 v[168:171], v[160:163], v[144:151], v164\n\tv_dot2c_f32_bf16 v72, v70, v81\n\tv_dot2c_f32_bf16 v73, v81, v81\n\t"
          "v_mfma_f32_32x32x16_bf16 v[0:15], v[152:155], v[112:115], v[0:15]\n\ts_waitcnt lgkmcnt(8)\n\tds_read_b128 v[48:51], v71 offset:32768\n\tds_read_b128 v[52:55], v71 offset:36864\n\t"
          "ds_read_b128 v[56:59], v85 offset:32768\n\tv_mfma_f32_32x32x16_bf16 v[16:31], v[152:155], v[116:119], v[16:31]\n\ts_waitcnt lgkmcnt(8)\n\tds_read_b128 v[60:63], v85 offset:36864\n\t"
          "ds_read_b128 v[76:79], v64 offset:32768\n\tv_dot2c_f32_bf16 v72, v70, v82\n\tv_dot2c_f32_bf16 v73, v82, v82\n\tv_mfma_f32_32x32x16_bf16 v[0:15], v[156:159], v[120:123], v[0:15]\n\t"
          "v_mfma_f32_32x32x16_bf16 v[16:31], v[156:159], v[124:127], v[16:31]\n\ts_add_u32 %[t0], %[mw], 0\n\ts_mov_b32 m0, %[t0]\n\ts_mul_i32 %[t0], %[rot], 196608\n\t"
          "buffer_load_dwordx4 %[vraw], %[descr], %[t0] offen offset:0 lds\n\tbuffer_load_dwordx4 %[vraw], %[descr], %[t0] offen offset:1024 lds\n\tbuffer_load_dwordx4 %[vraw], %[descr], %[t0] offen offset:2048 lds\n\ts_add_u32 %[t0], %[mx], 49152\n\t"
          "s_mov_b32 m0, %[t0]\n\ts_lshl_b32 %[t0], %[rot], 7\n\tbuffer_load_dwordx4 %[vxa], %[descx], %[t0] offen lds\n\ts_add_u32 %[rot], %[rot], 1\n\t"
          "s_and_b32 %[rot], %[rot], 7\n\tv_smfmac_f32_16x16x64_bf16 v[168:171], v[160:163], v[152:159], v164\n\tv_dot2c_f32_bf16 v72, v70, v83\n\tv_dot2c_f32_bf16 v73, v83, v83\n\t"
          "s_waitcnt vmcnt(4)\n\ts_waitcnt lgkmcnt(0)\n\ts_barrier\n\tv_mfma_f32_32x32x16_bf16 v[0:15], v[128:131], v[32:35], v[0:15]\n\t"
          "ds_read_b64_tr_b16 v[144:145], v84 offset:16384\n\tds_read_b64_tr_b16 v[146:147], v84 offset:16896\n\tds_read_b64_tr_b16 v[148:149], v84 offset:22528\n\tv_mfma_f32_32x32x16_bf16 v[16:31], v[128:131], v[36:39], v[16:31]\n\t"
          "ds_read_b64_tr_b16 v[150:151], v84 offset:23040\n\tds_read_b64_tr_b16 v[152:153], v84 offset:28672\n\tds_read_b64_tr_b16 v[154:155], v84 offset:29184\n\tv_dot2c_f32_bf16 v72, v70, v76\n\t"
          "v_dot2c_f32_bf16 v73, v76, v76\n\tv_mfma_f32_32x32x16_bf16 v[0:15], v[132:135], v[40:43], v[0:15]\n\tds_read_b64_tr_b16 v[156:157], v84 offset:34816\n\tds_read_b64_tr_b16 v[158:159], v84 offset:35328\n\t"
          "ds_read_b128 v[96:99], %[vr] offset:40960\n\tv_mfma_f32_32x32x16_bf16 v[16:31], v[132:135], v[44:47], v[16:31]\n\ts_waitcnt lgkmcnt(8)\n\tds_read_b128 v[100:103], %[vr] offset:45056\n\t"
          "ds_read_b128 v[104:107], v65 offset:40960\n\tds_read_b128 v[108:111], v65 offset:45056\n\tv_smfmac_f32_16x16x64_bf16 v[168:171], v[160:163], v[128:135], v164\n\tv_dot2c_f32_bf16 v72, v70, v77\n\t"
          "v_dot2c_f32_bf16 v73, v77, v77\n\tv_mfma_f32_32x32x16_bf16 v[0:15], v[136:139], v[48:51], v[0:15]\n\ts_waitcnt lgkmcnt(8)\n\tds_read_b128 v[112:115], v71 offset:40960\n\t"
          "ds_read_b128 v[116:119], v71 offset:45056\n\tds_read_b128 v[120:123], v85 offset:40960\n\tv_mfma_f32_32x32x16_bf16 v[16:31], v[136:139], v[52:55], v[16:31]\n\ts_waitcnt lgkmcnt(8)\n\t"
          "ds_read_b128 v[124:127], v85 offset:45056\n\tds_read_b128 v[80:83], v64 offset:40960\n\tv_dot2c_f32_bf16 v72, v70, v78\n\tv_dot2c_f32_bf16 v73, v78, v78\n\t"
          "v_mfma_f32_32x32x16_bf16 v[0:15], v[140:143], v[56:59], v[0:15]\n\tv_mfma_f32_32x32x16_bf16 v[16:31], v[140:143], v[60:63], v[16:31]\n\ts_add_u32 %[t0], %[mw], 24576\n\ts_mov_b32 m0, %[t0]\n\t"
          "s_mul_i32 %[t0], %[rot], 196608\n\tbuffer_load_dwordx4 %[vraw], %[descr], %[t0] offen offset:0 lds\n\tbuffer_load_dwordx4 %[vraw], %[descr], %[t0] offen offset:1024 lds\n\tbuffer_load_dwordx4 %[vraw], %[descr], %[t0] offen offset:2048 lds\n\t"
          "s_add_u32 %[t0], %[mx], 57344\n\ts_mov_b32 m0, %[t0]\n\ts_lshl_b32 %[t0], %[rot], 7\n\tbuffer_load_dwordx4 %[vxa], %[descx], %[t0] offen lds\n\t"
          "s_add_u32 %[rot], %[rot], 1\n\ts_and_b32 %[rot], %[rot], 7\n\tv_smfmac_f32_16x16x64_bf16 v[168:171], v[160:163], v[136:143], v164\n\tv_dot2c_f32_bf16 v72, v70, v79\n\t"
          "v_dot2c_f32_bf16 v73, v79, v79\n\ts_waitcnt vmcnt(4)\n\ts_waitcnt lgkmcnt(0)\n\ts_barrier\n\t"
          "v_lshrrev_b32 v75, 2, %[l16]\n\tv_and_b32 v75, 64, v75\n\tv_xor_b32 v75, v75, %[l16]\n\tv_and_b32 v75, 0x70, v75\n\t"
          "v_lshrrev_b32 v74, 7, %[l16]\n\tv_mad_u32_u24 v74, v74, %[jl2], v75\n\ts_lshl_b32 %[rot], %[jl2], 3\n\ts_add_u32 m0, %[mw], 49152\n\t"
          "s_mov_b32 %[t0], 0\n\tbuffer_load_dwordx4 v74, %[descj], %[t0] offen lds\n\ts_add_u32 m0, m0, 1024\n\ts_add_u32 %[t0], %[t0], %[rot]\n\t"
          "buffer_load_dwordx4 v74, %[descj], %[t0] offen lds\n\ts_add_u32 m0, m0, 1024\n\ts_add_u32 %[t0], %[t0], %[rot]\n\tbuffer_load_dwordx4 v74, %[descj], %[t0] offen lds\n\t"
          "s_add_u32 m0, m0, 1024\n\ts_add_u32 %[t0], %[t0], %[rot]\n\tv_mfma_f32_32x32x16_bf16 v[0:15], v[144:147], v[96:99], v[0:15]\n\tds_read_b64_tr_b16 v[128:129], %[vtr] offset:0\n\t"
          "ds_read_b64_tr_b16 v[130:131], %[vtr] offset:512\n\tds_read_b64_tr_b16 v[132:133], %[vtr] offset:6144\n\tv_mfma_f32_32x32x16_bf16 v[16:31], v[144:147], v[100:103], v[16:31]\n\tds_read_b64_tr_b16 v[134:135], %[vtr] offset:6656\n\t"
          "ds_read_b64_tr_b16 v[136:137], %[vtr] offset:12288\n\tds_read_b64_tr_b16 v[138:139], %[vtr] offset:12800\n\tv_dot2c_f32_bf16 v72, v70, v80\n\tv_dot2c_f32_bf16 v73, v80, v80\n\t"
          "v_mfma_f32_32x32x16_bf16 v[0:15], v[148:151], v[104:107], v[0:15]\n\tds_read_b64_tr_b16 v[140:141], %[vtr] offset:18432\n\tds_read_b64_tr_b16 v[142:143], %[vtr] offset:18944\n\tds_read_b128 v[32:35], %[vr] offset:49152\n\t"
          "v_mfma_f32_32x32x16_bf16 v[16:31], v[148:151], v[108:111], v[16:31]\n\ts_waitcnt lgkmcnt(8)\n\tds_read_b128 v[36:39], %[vr] offset:53248\n\tds_read_b128 v[40:43], v65 offset:49152\n\t"
          "ds_read_b128 v[44:47], v65 offset:53248\n\tv_smfmac_f32_16x16x64_bf16 v[168:171], v[160:163], v[144:151], v164\n\tv_dot2c_f32_bf16 v72, v70, v81\n\tv_dot2c_f32_bf16 v73, v81, v81\n\t"
          "v_mfma_f32_32x32x16_bf16 v[0:15], v[152:155], v[112:115], v[0:15]\n\ts_waitcnt lgkmcnt(8)\n\tds_read_b128 v[48:51], v71 offset:49152\n\tds_read_b128 v[52:55], v71 offset:53248\n\t"
          "ds_read_b128 v[56:59], v85 offset:49152\n\tv_mfma_f32_32x32x16_bf16 v[16:31], v[152:155], v[116:119], v[16:31]\n\ts_waitcnt lgkmcnt(8)\n\tds_read_b128 v[60:63], v85 offset:53248\n\t"
          "ds_read_b128 v[76:79], v64 offset:49152\n\tv_dot2c_f32_bf16 v72, v70, v82\n\tv_dot2c_f32_bf16 v73, v82, v82\n\tv_mfma_f32_32x32x16_bf16 v[0:15], v[156:159], v[120:123], v[0:15]\n\t"
          "v_mfma_f32_32x32x16_bf16 v[16:31], v[156:159], v[124:127], v[16:31]\n\tv_smfmac_f32_16x16x64_bf16 v[168:171], v[160:163], v[152:159], v164\n\tv_dot2c_f32_bf16 v72, v70, v83\n\tv_dot2c_f32_bf16 v73, v83, v83\n\t"
          "s_waitcnt vmcnt(3)\n\ts_waitcnt lgkmcnt(0)\n\ts_barrier\n\ts_mov_b32 m0, %[mw]\n\t"
          "s_nop 0\n\tbuffer_load_dwordx4 v74, %[descj], %[t0] offen lds\n\tv_mfma_f32_32x32x16_bf16 v[0:15], v[128:131], v[32:35], v[0:15]\n\tds_read_b128 v[80:83], v64 offset:57344\n\t"
          "ds_read_b64_tr_b16 v[144:145], %[vtr] offset:24576\n\tds_read_b64_tr_b16 v[146:147], %[vtr] offset:25088\n\tv_mfma_f32_32x32x16_bf16 v[16:31], v[128:131], v[36:39], v[16:31]\n\tds_read_b64_tr_b16 v[148:149], %[vtr] offset:30720\n\t"
          "ds_read_b64_tr_b16 v[150:151], %[vtr] offset:31232\n\tds_read_b64_tr_b16 v[152:153], %[vtr] offset:36864\n\tv_dot2c_f32_bf16 v72, v70, v76\n\tv_dot2c_f32_bf16 v73, v76, v76\n\t"
          "v_mfma_f32_32x32x16_bf16 v[0:15], v[132:135], v[40:43], v[0:15]\n\tds_read_b64_tr_b16 v[154:155], %[vtr] offset:37376\n\tds_read_b64_tr_b16 v[156:157], %[vtr] offset:43008\n\tds_read_b64_tr_b16 v[158:159], %[vtr] offset:43520\n\t"
          "v_mfma_f32_32x32x16_bf16 v[16:31], v[132:135], v[44:47], v[16:31]\n\ts_waitcnt lgkmcnt(8)\n\tds_read_b128 v[96:99], %[vr] offset:57344\n\tds_read_b128 v[100:103], %[vr] offset:61440\n\t"
          "ds_read_b128 v[104:107], v65 offset:57344\n\tv_smfmac_f32_16x16x64_bf16 v[168:171], v[160:163], v[128:135], v164\n\tv_dot2c_f32_bf16 v72, v70, v77\n\tv_dot2c_f32_bf16 v73, v77, v77\n\t"
          "v_mfma_f32_32x32x16_bf16 v[0:15], v[136:139], v[48:51], v[0:15]\n\ts_waitcnt lgkmcnt(8)\n\tds_read_b128 v[108:111], v65 offset:61440\n\tds_read_b128 v[112:115], v71 offset:57344\n\t"
          "ds_read_b128 v[116:119], v71 offset:61440\n\tv_mfma_f32_32x32x16_bf16 v[16:31], v[136:139], v[52:55], v[16:31]\n\ts_waitcnt lgkmcnt(8)\n\tds_read_b128 v[120:123], v85 offset:57344\n\t"
          "ds_read_b128 v[124:127], v85 offset:61440\n\tv_dot2c_f32_bf16 v72, v70, v78\n\tv_dot2c_f32_bf16 v73, v78, v78\n\tv_mfma_f32_32x32x16_bf16 v[0:15], v[140:143], v[56:59], v[0:15]\n\t"
          "v_mfma_f32_32x32x16_bf16 v[16:31], v[140:143], v[60:63], v[16:31]\n\tv_smfmac_f32_16x16x64_bf16 v[168:171], v[160:163], v[136:143], v164\n\tv_dot2c_f32_bf16 v72, v70, v79\n\tv_dot2c_f32_bf16 v73, v79, v79\n\t"
          "s_waitcnt lgkmcnt(8)\n\tv_dot2c_f32_bf16 v72, v70, v80\n\tv_dot2c_f32_bf16 v73, v80, v80\n\tv_dot2c_f32_bf16 v72, v70, v81\n\t"
          "v_dot2c_f32_bf16 v73, v81, v81\n\tv_dot2c_f32_bf16 v72, v70, v82\n\tv_dot2c_f32_bf16 v73, v82, v82\n\tv_dot2c_f32_bf16 v72, v70, v83\n\t"
          "v_dot2c_f32_bf16 v73, v83, v83\n\ts_nop 2\n\tds_write_b64 %[vsx], v[72:73]\n\ts_waitcnt lgkmcnt(0)\n\t"
          "s_barrier\n\tv_smfmac_f32_16x16x64_bf16 v[168:171], v[160:163], v[144:151], v164\n\tv_smfmac_f32_16x16x64_bf16 v[168:171], v[160:163], v[152:159], v164\n\tv_mfma_f32_32x32x16_bf16 v[0:15], v[144:147], v[96:99], v[0:15]\n\t"
          "ds_read_b128 v[32:35], %[vst] offset:0\n\tds_read_b128 v[36:39], %[vst] offset:512\n\tds_read_b128 v[40:43], %[vst] offset:1024\n\tds_read_b128 v[44:47], %[vst] offset:1536\n\t"
          "ds_read_b128 v[48:51], %[vst] offset:2048\n\tds_read_b128 v[52:55], %[vst] offset:2560\n\tds_read_b128 v[56:59], %[vst] offset:3072\n\tds_read_b128 v[60:63], %[vst] offset:3584\n\t"
          "v_add_f32 v168, v168, v169\n\tv_mfma_f32_32x32x16_bf16 v[16:31], v[144:147], v[100:103], v[16:31]\n\tv_add_f32 v170, v170, v171\n\ts_mov_b64 exec, 0xffff\n\t"
          "ds_write2_b32 %[vx], v168, v170 offset0:0 offset1:16\n\ts_mov_b64 exec, -1\n\ts_waitcnt lgkmcnt(7)\n\tv_add_f32 v32, v32, v36\n\t"
          "v_add_f32 v33, v33, v37\n\tv_add_f32 v34, v34, v38\n\tv_add_f32 v35, v35, v39\n\ts_waitcnt lgkmcnt(5)\n\t"
          "v_add_f32 v40, v40, v44\n\tv_mfma_f32_32x32x16_bf16 v[0:15], v[148:151], v[104:107], v[0:15]\n\tv_add_f32 v41, v41, v45\n\tv_add_f32 v42, v42, v46\n\t"
          "v_add_f32 v43, v43, v47\n\ts_waitcnt lgkmcnt(3)\n\tv_add_f32 v48, v48, v52\n\tv_add_f32 v49, v49, v53\n\t"
          "v_add_f32 v50, v50, v54\n\tv_add_f32 v51, v51, v55\n\ts_waitcnt lgkmcnt(1)\n\tv_mfma_f32_32x32x16_bf16 v[16:31], v[148:151], v[108:111], v[16:31]\n\t"
          "v_add_f32 v56, v56, v60\n\tv_add_f32 v57, v57, v61\n\tv_add_f32 v58, v58, v62\n\tv_add_f32 v59, v59, v63\n\t"
          "v_add_f32 v32, v32, v40\n\tv_add_f32 v33, v33, v41\n\tv_add_f32 v34, v34, v42\n\tv_add_f32 v35, v35, v43\n\t"
          "v_add_f32 v48, v48, v56\n\tv_mfma_f32_32x32x16_bf16 v[0:15], v[152:155], v[112:115], v[0:15]\n\tv_add_f32 v49, v49, v57\n\tv_add_f32 v50, v50, v58\n\t"
          "v_add_f32 v51, v51, v59\n\tv_add_f32 v32, v32, v48\n\tv_add_f32 v33, v33, v49\n\tv_add_f32 v34, v34, v50\n\t"
          "v_add_f32 v35, v35, v51\n\tv_mul_f32 v64, 0xbb000000, v32\n\tv_mul_f32 v66, 0xbb000000, v34\n\tv_mfma_f32_32x32x16_bf16 v[16:31], v[152:155], v[116:119], v[16:31]\n\t"
          "v_mul_f32 v68, 0x3b000000, v33\n\tv_mul_f32 v69, 0x3b000000, v35\n\tv_fma_f32 v68, -v64, v64, v68\n\tv_fma_f32 v69, -v66, v66, v69\n\t"
          "v_max_f32 v68, 0, v68\n\tv_max_f32 v69, 0, v69\n\tv_add_f32 v70, 0x3727c5ac, v68\n\tv_add_f32 v71, 0x3727c5ac, v69\n\t"
          "v_rsq_f32 v65, v70\n\tv_mfma_f32_32x32x16_bf16 v[0:15], v[156:159], v[120:123], v[0:15]\n\tv_rsq_f32 v67, v71\n\ts_nop 0\n\t"
          "ds_read_b128 v[32:35], %[vtab] offset:0\n\tds_read_b128 v[36:39], %[vtab] offset:32\n\tds_read_b128 v[40:43], %[vtab] offset:64\n\tds_read_b128 v[44:47], %[vtab] offset:96\n\t"
          "v_mul_f32 v65, %[sc], v65\n\tv_mul_f32 v67, %[sc], v67\n\tv_add_u32 v84, %[unit], %[vo]\n\tv_mfma_f32_32x32x16_bf16 v[16:31], v[156:159], v[124:127], v[16:31]\n\t"
          "s_lshl_b32 %[t0], %[unit], 6\n\ts_waitcnt lgkmcnt(0)\n\ts_nop 11\n\tv_fmac_f32 v0, v64, v32\n\t"
          "v_fmac_f32 v1, v64, v33\n\tv_fmac_f32 v2, v64, v34\n\tv_fmac_f32 v3, v64, v35\n\tv_fmac_f32 v4, v64, v36\n\t"
          "v_fmac_f32 v5, v64, v37\n\tv_fmac_f32 v6, v64, v38\n\tv_fmac_f32 v7, v64, v39\n\tv_fmac_f32 v8, v64, v40\n\t"
          "v_fmac_f32 v9, v64, v41\n\tv_fmac_f32 v10, v64, v42\n\tv_fmac_f32 v11, v64, v43\n\tv_fmac_f32 v12, v64, v44\n\t"
          "v_fmac_f32 v13, v64, v45\n\tv_fmac_f32 v14, v64, v46\n\tv_fmac_f32 v15, v64, v47\n\tv_mul_f32 v0, v65, v0\n\t"
          "v_mul_f32 v1, v65, v1\n\tv_mul_f32 v2, v65, v2\n\tv_mul_f32 v3, v65, v3\n\tv_mul_f32 v4, v65, v4\n\t"
          "v_mul_f32 v5, v65, v5\n\tv_mul_f32 v6, v65, v6\n\tv_mul_f32 v7, v65, v7\n\tv_mul_f32 v8, v65, v8\n\t"
          "v_mul_f32 v9, v65, v9\n\tv_mul_f32 v10, v65, v10\n\tv_mul_f32 v11, v65, v11\n\tv_mul_f32 v12, v65, v12\n\t"
          "v_mul_f32 v13, v65, v13\n\tv_mul_f32 v14, v65, v14\n\tv_mul_f32 v15, v65, v15\n\tv_cvt_pk_bf16_f32 v68, v0, v1\n\t"
          "v_cvt_pk_bf16_f32 v69, v2, v3\n\tv_cvt_pk_bf16_f32 v70, v4, v5\n\tv_cvt_pk_bf16_f32 v71, v6, v7\n\tv_cvt_pk_bf16_f32 v72, v8, v9\n\t"
          "v_cvt_pk_bf16_f32 v73, v10, v11\n\tv_cvt_pk_bf16_f32 v74, v12, v13\n\tv_cvt_pk_bf16_f32 v75, v14, v15\n\tbuffer_store_dwordx4 v[68:71], %[vo], %[desc], 0 offen sc1\n\t"
          "buffer_store_dwordx4 v[72:75], %[vo], %[desc], %[t0] offen sc1\n\tv_fmac_f32 v16, v66, v32\n\tv_fmac_f32 v17, v66, v33\n\tv_fmac_f32 v18, v66, v34\n\t"
          "v_fmac_f32 v19, v66, v35\n\tv_fmac_f32 v20, v66, v36\n\tv_fmac_f32 v21, v66, v37\n\tv_fmac_f32 v22, v66, v38\n\t"
          "v_fmac_f32 v23, v66, v39\n\tv_fmac_f32 v24, v66, v40\n\tv_fmac_f32 v25, v66, v41\n\tv_fmac_f32 v26, v66, v42\n\t"
          "v_fmac_f32 v27, v66, v43\n\tv_fmac_f32 v28, v66, v44\n\tv_fmac_f32 v29, v66, v45\n\tv_fmac_f32 v30, v66, v46\n\t"
          "v_fmac_f32 v31, v66, v47\n\tv_mul_f32 v16, v67, v16\n\tv_mul_f32 v17, v67, v17\n\tv_mul_f32 v18, v67, v18\n\t"
          "v_mul_f32 v19, v67, v19\n\tv_mul_f32 v20, v67, v20\n\tv_mul_f32 v21, v67, v21\n\tv_mul_f32 v22, v67, v22\n\t"
          "v_mul_f32 v23, v67, v23\n\tv_mul_f32 v24, v67, v24\n\tv_mul_f32 v25, v67, v25\n\tv_mul_f32 v26, v67, v26\n\t"
          "v_mul_f32 v27, v67, v27\n\tv_mul_f32 v28, v67, v28\n\tv_mul_f32 v29, v67, v29\n\tv_mul_f32 v30, v67, v30\n\t"
          "v_mul_f32 v31, v67, v31\n\tv_cvt_pk_bf16_f32 v76, v16, v17\n\tv_cvt_pk_bf16_f32 v77, v18, v19\n\tv_cvt_pk_bf16_f32 v78, v20, v21\n\t"
          "v_cvt_pk_bf16_f32 v79, v22, v23\n\tv_cvt_pk_bf16_f32 v80, v24, v25\n\tv_cvt_pk_bf16_f32 v81, v26, v27\n\tv_cvt_pk_bf16_f32 v82, v28, v29\n\t"
          "v_cvt_pk_bf16_f32 v83, v30, v31\n\tbuffer_store_dwordx4 v[76:79], v84, %[desc], 0 offen sc1\n\tbuffer_store_dwordx4 v[80:83], v84, %[desc], %[t0] offen sc1\n\ts_branch RX%=\n\t"
          "RV%=:\n\ts_add_u32 %[t0], %[mw], 24576\n\ts_mov_b32 m0, %[t0]\n\ts_mul_i32 %[t0], %[rot], 196608\n\t"
          "buffer_load_dwordx4 %[vraw], %[descr], %[t0] offen offset:0 lds\n\tbuffer_load_dwordx4 %[vraw], %[descr], %[t0] offen offset:1024 lds\n\tbuffer_load_dwordx4 %[vraw], %[descr], %[t0] offen offset:2048 lds\n\ts_add_u32 %[t0], %[mx], 8192\n\t"
          "s_mov_b32 m0, %[t0]\n\ts_lshl_b32 %[t0], %[rot], 7\n\tbuffer_load_dwordx4 %[vxa], %[descx], %[t0] offen lds\n\ts_add_u32 %[rot], %[rot], 1\n\t"
          "s_and_b32 %[rot], %[rot], 7\n\tv_mov_b32 v68, 0\n\tv_mov_b32 v69, 0\n\tv_mov_b32 v70, 0x3f803f80\n\t"
          "v_mov_b32 v72, 0\n\tv_mov_b32 v73, 0\n\tv_add_u32 v84, 0x8000, %[vtr]\n\tv_add_u32 v64, %[mx], %[l16]\n\t"
          "v_xor_b32 v65, 32, %[vr]\n\tv_xor_b32 v71, 64, %[vr]\n\tv_xor_b32 v85, 96, %[vr]\n\tv_and_b32 v164, 0xe0, %[l16]\n\t"
          "v_mov_b32 v161, 0x3f803f80\n\tv_cmp_eq_u32 vcc, 0, v164\n\tv_mov_b32 v168, 0\n\tv_mov_b32 v169, 0\n\t"
          "v_cndmask_b32 v160, 0, v161, vcc\n\tv_cmp_eq_u32 vcc, 32, v164\n\tv_mov_b32 v170, 0\n\tv_and_b32 v164, 16, %[l16]\n\t"
          "v_cndmask_b32 v162, 0, v161, vcc\n\tv_cmp_eq_u32 vcc, 0, v164\n\tv_mov_b32 v161, 0x4444\n\tv_mov_b32 v164, 0xeeee\n\t"
          "v_cndmask_b32 v164, v164, v161, vcc\n\tv_mov_b32 v161, v160\n\tv_mov_b32 v163, v162\n\tv_mov_b32 v171, 0\n\t"
          "s_nop 1\n\ts_waitcnt vmcnt(4)\n\ts_waitcnt lgkmcnt(0)\n\ts_barrier\n\t"
          "ds_read_b64_tr_b16 v[128:129], %[vtr] offset:0\n\tds_read_b64_tr_b16 v[130:131], %[vtr] offset:512\n\tds_read_b64_tr_b16 v[132:133], %[vtr] offset:6144\n\tds_read_b64_tr_b16 v[134:135], %[vtr] offset:6656\n\t"
          "ds_read_b64_tr_b16 v[136:137], %[vtr] offset:12288\n\tds_read_b64_tr_b16 v[138:139], %[vtr] offset:12800\n\tds_read_b64_tr_b16 v[140:141], %[vtr] offset:18432\n\tds_read_b64_tr_b16 v[142:143], %[vtr] offset:18944\n\t"
          "s_waitcnt lgkmcnt(4)\n\tds_read_b128 v[32:35], %[vr] offset:0\n\tds_read_b128 v[36:39], v65 offset:0\n\tds_read_b128 v[40:43], v71 offset:0\n\t"
          "ds_read_b128 v[44:47], v85 offset:0\n\tds_read_b128 v[76:79], v64 offset:0\n\ts_add_u32 %[t0], %[mw], 49152\n\ts_mov_b32 m0, %[t0]\n\t"
          "s_mul_i32 %[t0], %[rot], 196608\n\tbuffer_load_dwordx4 %[vraw], %[descr], %[t0] offen offset:0 lds\n\tbuffer_load_dwordx4 %[vraw], %[descr], %[t0] offen offset:1024 lds\n\tbuffer_load_dwordx4 %[vraw], %[descr], %[t0] offen offset:2048 lds\n\t"
          "s_add_u32 %[t0], %[mx], 16384\n\ts_mov_b32 m0, %[t0]\n\ts_lshl_b32 %[t0], %[rot], 7\n\tbuffer_load_dwordx4 %[vxa], %[descx], %[t0] offen lds\n\t"
          "s_add_u32 %[rot], %[rot], 1\n\ts_and_b32 %[rot], %[rot], 7\n\ts_waitcnt vmcnt(4)\n\ts_waitcnt lgkmcnt(0)\n\t"
          "s_barrier\n\ts_add_u32 %[t0], %[mw], 0\n\ts_mov_b32 m0, %[t0]\n\ts_mul_i32 %[t0], %[rot], 196608\n\t"
          "buffer_load_dwordx4 %[vraw], %[descr], %[t0] offen offset:0 lds\n\tbuffer_load_dwordx4 %[vraw], %[descr], %[t0] offen offset:1024 lds\n\tbuffer_load_dwordx4 %[vraw], %[descr], %[t0] offen offset:2048 lds\n\ts_add_u32 %[t0], %[mx], 24576\n\t"
          "s_mov_b32 m0, %[t0]\n\ts_lshl_b32 %[t0], %[rot], 7\n\tbuffer_load_dwordx4 %[vxa], %[descx], %[t0] offen lds\n\ts_add_u32 %[rot], %[rot], 1\n\t"
          "s_and_b32 %[rot], %[rot], 7\n\tv_mfma_f32_32x32x16_bf16 v[0:15], v[32:35], v[128:131], 0\n\tds_read_b64_tr_b16 v[144:145], %[vtr] offset:24576\n\tds_read_b64_tr_b16 v[146:147], %[vtr] offset:25088\n\t"
          "ds_read_b64_tr_b16 v[148:149], %[vtr] offset:30720\n\tds_read_b64_tr_b16 v[150:151], %[vtr] offset:31232\n\tv_dot2c_f32_bf16 v72, v70, v76\n\tv_dot2c_f32_bf16 v73, v76, v76\n\t"
          "v_mfma_f32_32x32x16_bf16 v[0:15], v[36:39], v[132:135], v[0:15]\n\tds_read_b64_tr_b16 v[152:153], %[vtr] offset:36864\n\tds_read_b64_tr_b16 v[154:155], %[vtr] offset:37376\n\tds_read_b64_tr_b16 v[156:157], %[vtr] offset:43008\n\t"
          "ds_read_b64_tr_b16 v[158:159], %[vtr] offset:43520\n\tv_smfmac_f32_16x16x64_bf16 v[168:171], v[160:163], v[128:135], v164\n\tv_dot2c_f32_bf16 v72, v70, v77\n\tv_dot2c_f32_bf16 v73, v77, v77\n\t"
          "v_mfma_f32_32x32x16_bf16 v[0:15], v[40:43], v[136:139], v[0:15]\n\tds_read_b128 v[96:99], %[vr] offset:8192\n\tds_read_b128 v[100:103], v65 offset:8192\n\tds_read_b128 v[104:107], v71 offset:8192\n\t"
          "ds_read_b128 v[108:111], v85 offset:8192\n\tv_dot2c_f32_bf16 v72, v70, v78\n\tv_dot2c_f32_bf16 v73, v78, v78\n\tv_mfma_f32_32x32x16_bf16 v[0:15], v[44:47], v[140:143], v[0:15]\n\t"
          "s_waitcnt lgkmcnt(8)\n\tds_read_b128 v[80:83], v64 offset:8192\n\tv_smfmac_f32_16x16x64_bf16 v[168:171], v[160:163], v[136:143], v164\n\tv_dot2c_f32_bf16 v72, v70, v79\n\t"
          "v_dot2c_f32_bf16 v73, v79, v79\n\ts_waitcnt vmcnt(4)\n\ts_waitcnt lgkmcnt(0)\n\ts_barrier\n\t"
          "s_add_u32 %[t0], %[mw], 24576\n\ts_mov_b32 m0, %[t0]\n\ts_mul_i32 %[t0], %[rot], 196608\n\tbuffer_load_dwordx4 %[vraw], %[descr], %[t0] offen offset:0 lds\n\t"
          "buffer_load_dwordx4 %[vraw], %[descr], %[t0] offen offset:1024 lds\n\tbuffer_load_dwordx4 %[vraw], %[descr], %[t0] offen offset:2048 lds\n\ts_add_u32 %[t0], %[mx], 32768\n\ts_mov_b32 m0, %[t0]\n\t"
          "s_lshl_b32 %[t0], %[rot], 7\n\tbuffer_load_dwordx4 %[vxa], %[descx], %[t0] offen lds\n\ts_add_u32 %[rot], %[rot], 1\n\ts_and_b32 %[rot], %[rot], 7\n\t"
          "v_mfma_f32_32x32x16_bf16 v[0:15], v[96:99], v[144:147], v[0:15]\n\tds_read_b64_tr_b16 v[128:129], v84 offset:16384\n\tds_read_b64_tr_b16 v[130:131], v84 offset:16896\n\tds_read_b64_tr_b16 v[132:133], v84 offset:22528\n\t"
          "ds_read_b64_tr_b16 v[134:135], v84 offset:23040\n\tv_dot2c_f32_bf16 v72, v70, v80\n\tv_dot2c_f32_bf16 v73, v80, v80\n\tv_mfma_f32_32x32x16_bf16 v[0:15], v[100:103], v[148:151], v[0:15]\n\t"
          "ds_read_b64_tr_b16 v[136:137], v84 offset:28672\n\tds_read_b64_tr_b16 v[138:139], v84 offset:29184\n\tds_read_b64_tr_b16 v[140:141], v84 offset:34816\n\tds_read_b64_tr_b16 v[142:143], v84 offset:35328\n\t"
          "v_smfmac_f32_16x16x64_bf16 v[168:171], v[160:163], v[144:151], v164\n\tv_dot2c_f32_bf16 v72, v70, v81\n\tv_dot2c_f32_bf16 v73, v81, v81\n\tv_mfma_f32_32x32x16_bf16 v[0:15], v[104:107], v[152:155], v[0:15]\n\t"
          "ds_read_b128 v[32:35], %[vr] offset:16384\n\tds_read_b128 v[36:39], v65 offset:16384\n\tds_read_b128 v[40:43], v71 offset:16384\n\tds_read_b128 v[44:47], v85 offset:16384\n\t"
          "v_dot2c_f32_bf16 v72, v70, v82\n\tv_dot2c_f32_bf16 v73, v82, v82\n\tv_mfma_f32_32x32x16_bf16 v[0:15], v[108:111], v[156:159], v[0:15]\n\ts_waitcnt lgkmcnt(8)\n\t"
          "ds_read_b128 v[76:79], v64 offset:16384\n\tv_smfmac_f32_16x16x64_bf16 v[168:171], v[160:163], v[152:159], v164\n\tv_dot2c_f32_bf16 v72, v70, v83\n\tv_dot2c_f32_bf16 v73, v83, v83\n\t"
          "s_waitcnt vmcnt(4)\n\ts_waitcnt lgkmcnt(0)\n\ts_barrier\n\ts_add_u32 %[t0], %[mw], 49152\n\t"
          "s_mov_b32 m0, %[t0]\n\ts_mul_i32 %[t0], %[rot], 196608\n\tbuffer_load_dwordx4 %[vraw], %[descr], %[t0] offen offset:0 lds\n\tbuffer_load_dwordx4 %[vraw], %[descr], %[t0] offen offset:1024 lds\n\t"
          "buffer_load_dwordx4 %[vraw], %[descr], %[t0] offen offset:2048 lds\n\ts_add_u32 %[t0], %[mx], 40960\n\ts_mov_b32 m0, %[t0]\n\ts_lshl_b32 %[t0], %[rot], 7\n\t"
          "buffer_load_dwordx4 %[vxa], %[descx], %[t0] offen lds\n\ts_add_u32 %[rot], %[rot], 1\n\ts_and_b32 %[rot], %[rot], 7\n\tv_mfma_f32_32x32x16_bf16 v[0:15], v[32:35], v[128:131], v[0:15]\n\t"
          "ds_read_b64_tr_b16 v[144:145], %[vtr] offset:0\n\tds_read_b64_tr_b16 v[146:147], %[vtr] offset:512\n\tds_read_b64_tr_b16 v[148:149], %[vtr] offset:6144\n\tds_read_b64_tr_b16 v[150:151], %[vtr] offset:6656\n\t"
          "v_dot2c_f32_bf16 v72, v70, v76\n\tv_dot2c_f32_bf16 v73, v76, v76\n\tv_mfma_f32_32x32x16_bf16 v[0:15], v[36:39], v[132:135], v[0:15]\n\tds_read_b64_tr_b16 v[152:153], %[vtr] offset:12288\n\t"
          "ds_read_b64_tr_b16 v[154:155], %[vtr] offset:12800\n\tds_read_b64_tr_b16 v[156:157], %[vtr] offset:18432\n\tds_read_b64_tr_b16 v[158:159], %[vtr] offset:18944\n\tv_smfmac_f32_16x16x64_bf16 v[168:171], v[160:163], v[128:135], v164\n\t"
          "v_dot2c_f32_bf16 v72, v70, v77\n\tv_dot2c_f32_bf16 v73, v77, v77\n\tv_mfma_f32_32x32x16_bf16 v[0:15], v[40:43], v[136:139], v[0:15]\n\tds_read_b128 v[96:99], %[vr] offset:24576\n\t"
          "ds_read_b128 v[100:103], v65 offset:24576\n\tds_read_b128 v[104:107], v71 offset:24576\n\tds_read_b128 v[108:111], v85 offset:24576\n\tv_dot2c_f32_bf16 v72, v70, v78\n\t"
          "v_dot2c_f32_bf16 v73, v78, v78\n\tv_mfma_f32_32x32x16_bf16 v[0:15], v[44:47], v[140:143], v[0:15]\n\ts_waitcnt lgkmcnt(8)\n\tds_read_b128 v[80:83], v64 offset:24576\n\t"
          "v_smfmac_f32_16x16x64_bf16 v[168:171], v[160:163], v[136:143], v164\n\tv_dot2c_f32_bf16 v72, v70, v79\n\tv_dot2c_f32_bf16 v73, v79, v79\n\ts_waitcnt vmcnt(4)\n\t"
          "s_waitcnt lgkmcnt(0)\n\ts_barrier\n\ts_add_u32 %[t0], %[mw], 0\n\ts_mov_b32 m0, %[t0]\n\t"
          "s_mul_i32 %[t0], %[rot], 196608\n\tbuffer_load_dwordx4 %[vraw], %[descr], %[t0] offen offset:0 lds\n\tbuffer_load_dwordx4 %[vraw], %[descr], %[t0] offen offset:1024 lds\n\tbuffer_load_dwordx4 %[vraw], %[descr], %[t0] offen offset:2048 lds\n\t"
          "s_add_u32 %[t0], %[mx], 49152\n\ts_mov_b32 m0, %[t0]\n\ts_lshl_b32 %[t0], %[rot], 7\n\tbuffer_load_dwordx4 %[vxa], %[descx], %[t0] offen lds\n\t"
          "s_add_u32 %[rot], %[rot], 1\n\ts_and_b32 %[rot], %[rot], 7\n\tv_mfma_f32_32x32x16_bf16 v[0:15], v[96:99], v[144:147], v[0:15]\n\tds_read_b64_tr_b16 v[128:129], %[vtr] offset:24576\n\t"
          "ds_read_b64_tr_b16 v[130:131], %[vtr] offset:25088\n\tds_read_b64_tr_b16 v[132:133], %[vtr] offset:30720\n\tds_read_b64_tr_b16 v[134:135], %[vtr] offset:31232\n\tv_dot2c_f32_bf16 v72, v70, v80\n\t"
          "v_dot2c_f32_bf16 v73, v80, v80\n\tv_mfma_f32_32x32x16_bf16 v[0:15], v[100:103], v[148:151], v[0:15]\n\tds_read_b64_tr_b16 v[136:137], %[vtr] offset:36864\n\tds_read_b64_tr_b16 v[138:139], %[vtr] offset:37376\n\t"
          "ds_read_b64_tr_b16 v[140:141], %[vtr] offset:43008\n\tds_read_b64_tr_b16 v[142:143], %[vtr] offset:43520\n\tv_smfmac_f32_16x16x64_bf16 v[168:171], v[160:163], v[144:151], v164\n\tv_dot2c_f32_bf16 v72, v70, v81\n\t"
          "v_dot2c_f32_bf16 v73, v81, v81\n\tv_mfma_f32_32x32x16_bf16 v[0:15], v[104:107], v[152:155], v[0:15]\n\tds_read_b128 v[32:35], %[vr] offset:32768\n\tds_read_b128 v[36:39], v65 offset:32768\n\t"
          "ds_read_b128 v[40:43], v71 offset:32768\n\tds_read_b128 v[44:47], v85 offset:32768\n\tv_dot2c_f32_bf16 v72, v70, v82\n\tv_dot2c_f32_bf16 v73, v82, v82\n\t"
          "v_mfma_f32_32x32x16_bf16 v[0:15], v[108:111], v[156:159], v[0:15]\n\ts_waitcnt lgkmcnt(8)\n\tds_read_b128 v[76:79], v64 offset:32768\n\tv_smfmac_f32_16x16x64_bf16 v[168:171], v[160:163], v[152:159], v164\n\t"
          "v_dot2c_f32_bf16 v72, v70, v83\n\tv_dot2c_f32_bf16 v73, v83, v83\n\ts_waitcnt vmcnt(4)\n\ts_waitcnt lgkmcnt(0)\n\t"
          "s_barrier\n\ts_add_u32 %[t0], %[mw], 24576\n\ts_mov_b32 m0, %[t0]\n\ts_mul_i32 %[t0], %[rot], 196608\n\t"
          "buffer_load_dwordx4 %[vraw], %[descr], %[t0] offen offset:0 lds\n\tbuffer_load_dwordx4 %[vraw], %[descr], %[t0] offen offset:1024 lds\n\tbuffer_load_dwordx4 %[vraw], %[descr], %[t0] offen offset:2048 lds\n\ts_add_u32 %[t0], %[mx], 57344\n\t"
          "s_mov_b32 m0, %[t0]\n\ts_lshl_b32 %[t0], %[rot], 7\n\tbuffer_load_dwordx4 %[vxa], %[descx], %[t0] offen lds\n\ts_add_u32 %[rot], %[rot], 1\n\t"
          "s_and_b32 %[rot], %[rot], 7\n\tv_mfma_f32_32x32x16_bf16 v[0:15], v[32:35], v[128:131], v[0:15]\n\tds_read_b64_tr_b16 v[144:145], v84 offset:16384\n\tds_read_b64_tr_b16 v[146:147], v84 offset:16896\n\t"
          "ds_read_b64_tr_b16 v[148:149], v84 offset:22528\n\tds_read_b64_tr_b16 v[150:151], v84 offset:23040\n\tv_dot2c_f32_bf16 v72, v70, v76\n\tv_dot2c_f32_bf16 v73, v76, v76\n\t"
          "v_mfma_f32_32x32x16_bf16 v[0:15], v[36:39], v[132:135], v[0:15]\n\tds_read_b64_tr_b16 v[152:153], v84 offset:28672\n\tds_read_b64_tr_b16 v[154:155], v84 offset:29184\n\tds_read_b64_tr_b16 v[156:157], v84 offset:34816\n\t"
          "ds_read_b64_tr_b16 v[158:159], v84 offset:35328\n\tv_smfmac_f32_16x16x64_bf16 v[168:171], v[160:163], v[128:135], v164\n\tv_dot2c_f32_bf16 v72, v70, v77\n\tv_dot2c_f32_bf16 v73, v77, v77\n\t"
          "v_mfma_f32_32x32x16_bf16 v[0:15], v[40:43], v[136:139], v[0:15]\n\tds_read_b128 v[96:99], %[vr] offset:40960\n\tds_read_b128 v[100:103], v65 offset:40960\n\tds_read_b128 v[104:107], v71 offset:40960\n\t"
          "ds_read_b128 v[108:111], v85 offset:40960\n\tv_dot2c_f32_bf16 v72, v70, v78\n\tv_dot2c_f32_bf16 v73, v78, v78\n\tv_mfma_f32_32x32x16_bf16 v[0:15], v[44:47], v[140:143], v[0:15]\n\t"
          "s_waitcnt lgkmcnt(8)\n\tds_read_b128 v[80:83], v64 offset:40960\n\tv_smfmac_f32_16x16x64_bf16 v[168:171], v[160:163], v[136:143], v164\n\tv_dot2c_f32_bf16 v72, v70, v79\n\t"
          "v_dot2c_f32_bf16 v73, v79, v79\n\ts_waitcnt vmcnt(4)\n\ts_waitcnt lgkmcnt(0)\n\ts_barrier\n\t"
          "v_lshrrev_b32 v75, 2, %[l16]\n\tv_and_b32 v75, 64, v75\n\tv_xor_b32 v75, v75, %[l16]\n\tv_and_b32 v75, 0x70, v75\n\t"
          "v_lshrrev_b32 v74, 7, %[l16]\n\tv_mad_u32_u24 v74, v74, %[jl2], v75\n\ts_lshl_b32 %[rot], %[jl2], 3\n\ts_add_u32 m0, %[mw], 49152\n\t"
          "s_mov_b32 %[t0], 0\n\tbuffer_load_dwordx4 v74, %[descj], %[t0] offen lds\n\ts_add_u32 m0, m0, 1024\n\ts_add_u32 %[t0], %[t0], %[rot]\n\t"
          "buffer_load_dwordx4 v74, %[descj], %[t0] offen lds\n\ts_add_u32 m0, m0, 1024\n\ts_add_u32 %[t0], %[t0], %[rot]\n\tbuffer_load_dwordx4 v74, %[descj], %[t0] offen lds\n\t"
          "s_add_u32 m0, m0, 1024\n\ts_add_u32 %[t0], %[t0], %[rot]\n\tv_mfma_f32_32x32x16_bf16 v[0:15], v[96:99], v[144:147], v[0:15]\n\tds_read_b64_tr_b16 v[128:129], %[vtr] offset:0\n\t"
          "ds_read_b64_tr_b16 v[130:131], %[vtr] offset:512\n\tds_read_b64_tr_b16 v[132:133], %[vtr] offset:6144\n\tds_read_b64_tr_b16 v[134:135], %[vtr] offset:6656\n\tv_dot2c_f32_bf16 v72, v70, v80\n\t"
          "v_dot2c_f32_bf16 v73, v80, v80\n\tv_mfma_f32_32x32x16_bf16 v[0:15], v[100:103], v[148:151], v[0:15]\n\tds_read_b64_tr_b16 v[136:137], %[vtr] offset:12288\n\tds_read_b64_tr_b16 v[138:139], %[vtr] offset:12800\n\t"
          "ds_read_b64_tr_b16 v[140:141], %[vtr] offset:18432\n\tds_read_b64_tr_b16 v[142:143], %[vtr] offset:18944\n\tv_smfmac_f32_16x16x64_bf16 v[168:171], v[160:163], v[144:151], v164\n\tv_dot2c_f32_bf16 v72, v70, v81\n\t"
          "v_dot2c_f32_bf16 v73, v81, v81\n\tv_mfma_f32_32x32x16_bf16 v[0:15], v[104:107], v[152:155], v[0:15]\n\tds_read_b128 v[32:35], %[vr] offset:49152\n\tds_read_b128 v[36:39], v65 offset:49152\n\t"
          "ds_read_b128 v[40:43], v71 offset:49152\n\tds_read_b128 v[44:47], v85 offset:49152\n\tv_dot2c_f32_bf16 v72, v70, v82\n\tv_dot2c_f32_bf16 v73, v82, v82\n\t"
          "v_mfma_f32_32x32x16_bf16 v[0:15], v[108:111], v[156:159], v[0:15]\n\ts_waitcnt lgkmcnt(8)\n\tds_read_b128 v[76:79], v64 offset:49152\n\tv_smfmac_f32_16x16x64_bf16 v[168:171], v[160:163], v[152:159], v164\n\t"
          "v_dot2c_f32_bf16 v72, v70, v83\n\tv_dot2c_f32_bf16 v73, v83, v83\n\ts_waitcnt vmcnt(3)\n\ts_waitcnt lgkmcnt(0)\n\t"
          "s_barrier\n\ts_mov_b32 m0, %[mw]\n\ts_nop 0\n\tbuffer_load_dwordx4 v74, %[descj], %[t0] offen lds\n\t"
          "v_mfma_f32_32x32x16_bf16 v[0:15], v[32:35], v[128:131], v[0:15]\n\tds_read_b128 v[80:83], v64 offset:57344\n\tds_read_b64_tr_b16 v[144:145], %[vtr] offset:24576\n\tds_read_b64_tr_b16 v[146:147], %[vtr] offset:25088\n\t"
          "ds_read_b64_tr_b16 v[148:149], %[vtr] offset:30720\n\tv_dot2c_f32_bf16 v72, v70, v76\n\tv_dot2c_f32_bf16 v73, v76, v76\n\tv_mfma_f32_32x32x16_bf16 v[0:15], v[36:39], v[132:135], v[0:15]\n\t"
          "ds_read_b64_tr_b16 v[150:151], %[vtr] offset:31232\n\tds_read_b64_tr_b16 v[152:153], %[vtr] offset:36864\n\tds_read_b64_tr_b16 v[154:155], %[vtr] offset:37376\n\tds_read_b64_tr_b16 v[156:157], %[vtr] offset:43008\n\t"
          "v_smfmac_f32_16x16x64_bf16 v[168:171], v[160:163], v[128:135], v164\n\tv_dot2c_f32_bf16 v72, v70, v77\n\tv_dot2c_f32_bf16 v73, v77, v77\n\tv_mfma_f32_32x32x16_bf16 v[0:15], v[40:43], v[136:139], v[0:15]\n\t"
          "ds_read_b64_tr_b16 v[158:159], %[vtr] offset:43520\n\tds_read_b128 v[96:99], %[vr] offset:57344\n\tds_read_b128 v[100:103], v65 offset:57344\n\tds_read_b128 v[104:107], v71 offset:57344\n\t"
          "v_dot2c_f32_bf16 v72, v70, v78\n\tv_dot2c_f32_bf16 v73, v78, v78\n\tv_mfma_f32_32x32x16_bf16 v[0:15], v[44:47], v[140:143], v[0:15]\n\ts_waitcnt lgkmcnt(8)\n\t"
          "ds_read_b128 v[108:111], v85 offset:57344\n\tv_smfmac_f32_16x16x64_bf16 v[168:171], v[160:163], v[136:143], v164\n\tv_dot2c_f32_bf16 v72, v70, v79\n\tv_dot2c_f32_bf16 v73, v79, v79\n\t"
          "s_waitcnt lgkmcnt(8)\n\tv_dot2c_f32_bf16 v72, v70, v80\n\tv_dot2c_f32_bf16 v73, v80, v80\n\tv_dot2c_f32_bf16 v72, v70, v81\n\t"
          "v_dot2c_f32_bf16 v73, v81, v81\n\tv_dot2c_f32_bf16 v72, v70, v82\n\tv_dot2c_f32_bf16 v73, v82, v82\n\tv_dot2c_f32_bf16 v72, v70, v83\n\t"
          "v_dot2c_f32_bf16 v73, v83, v83\n\ts_nop 2\n\tds_write_b64 %[vsx], v[72:73]\n\ts_waitcnt lgkmcnt(0)\n\t"
          "s_barrier\n\tv_smfmac_f32_16x16x64_bf16 v[168:171], v[160:163], v[144:151], v164\n\tv_smfmac_f32_16x16x64_bf16 v[168:171], v[160:163], v[152:159], v164\n\tv_mfma_f32_32x32x16_bf16 v[0:15], v[96:99], v[144:147], v[0:15]\n\t"
          "ds_read_b128 v[32:35], %[vst] offset:0\n\tds_read_b128 v[36:39], %[vst] offset:512\n\tds_read_b128 v[40:43], %[vst] offset:1024\n\tds_read_b128 v[44:47], %[vst] offset:1536\n\t"
          "ds_read_b128 v[48:51], %[vst] offset:2048\n\tds_read_b128 v[52:55], %[vst] offset:2560\n\tds_read_b128 v[56:59], %[vst] offset:3072\n\tds_read_b128 v[60:63], %[vst] offset:3584\n\t"
          "v_add_f32 v168, v168, v169\n\tv_add_f32 v170, v170, v171\n\ts_mov_b64 exec, 0xffff\n\tds_write2_b32 %[vx], v168, v170 offset0:0 offset1:16\n\t"
          "s_mov_b64 exec, -1\n\ts_waitcnt lgkmcnt(7)\n\tv_add_f32 v32, v32, v36\n\tv_add_f32 v33, v33, v37\n\t"
          "v_add_f32 v34, v34, v38\n\tv_add_f32 v35, v35, v39\n\ts_waitcnt lgkmcnt(5)\n\tv_add_f32 v40, v40, v44\n\t"
          "v_mfma_f32_32x32x16_bf16 v[0:15], v[100:103], v[148:151], v[0:15]\n\tv_add_f32 v41, v41, v45\n\tv_add_f32 v42, v42, v46\n\tv_add_f32 v43, v43, v47\n\t"
          "s_waitcnt lgkmcnt(3)\n\tv_add_f32 v48, v48, v52\n\tv_add_f32 v49, v49, v53\n\tv_add_f32 v50, v50, v54\n\t"
          "v_add_f32 v51, v51, v55\n\ts_waitcnt lgkmcnt(1)\n\tv_add_f32 v56, v56, v60\n\tv_add_f32 v57, v57, v61\n\t"
          "v_add_f32 v58, v58, v62\n\tv_add_f32 v59, v59, v63\n\tv_add_f32 v32, v32, v40\n\tv_add_f32 v33, v33, v41\n\t"
          "v_add_f32 v34, v34, v42\n\tv_add_f32 v35, v35, v43\n\tv_add_f32 v48, v48, v56\n\tv_mfma_f32_32x32x16_bf16 v[0:15], v[104:107], v[152:155], v[0:15]\n\t"
          "v_add_f32 v49, v49, v57\n\tv_add_f32 v50, v50, v58\n\tv_add_f32 v51, v51, v59\n\tv_add_f32 v32, v32, v48\n\t"
          "v_add_f32 v33, v33, v49\n\tv_add_f32 v34, v34, v50\n\tv_add_f32 v35, v35, v51\n\tv_mul_f32 v64, 0xbb000000, v32\n\t"
          "v_mul_f32 v66, 0xbb000000, v34\n\tv_mul_f32 v68, 0x3b000000, v33\n\tv_mul_f32 v69, 0x3b000000, v35\n\tv_fma_f32 v68, -v64, v64, v68\n\t"
          "v_fma_f32 v69, -v66, v66, v69\n\tv_max_f32 v68, 0, v68\n\tv_max_f32 v69, 0, v69\n\tv_add_f32 v70, 0x3727c5ac, v68\n\t"
          "v_add_f32 v71, 0x3727c5ac, v69\n\tv_rsq_f32 v65, v70\n\tv_mfma_f32_32x32x16_bf16 v[0:15], v[108:111], v[156:159], v[0:15]\n\tv_rsq_f32 v67, v71\n\t"
          "s_nop 0\n\ts_bitcmp0_b32 %[w], 1\n\ts_cbranch_scc1 RS%=\n\tv_mov_b32 v64, v66\n\t"
          "v_mov_b32 v65, v67\n\tRS%=:\n\tds_write_b32 %[vx], v64 offset:384\n\tds_write_b32 %[vx], v65 offset:512\n\t"
          "ds_read_b128 v[32:35], %[vtab] offset:384\n\tds_read_b128 v[36:39], %[vtab] offset:416\n\tds_read_b128 v[40:43], %[vtab] offset:448\n\tds_read_b128 v[44:47], %[vtab] offset:480\n\t"
          "ds_read_b128 v[48:51], %[vtab] offset:512\n\tds_read_b128 v[52:55], %[vtab] offset:544\n\tds_read_b128 v[56:59], %[vtab] offset:576\n\tds_read_b128 v[60:63], %[vtab] offset:608\n\t"
          "ds_read_b32 v64, %[vx]\n\ts_waitcnt lgkmcnt(0)\n\ts_nop 11\n\tv_pk_fma_f32 v[0:1], v[64:65], v[32:33], v[0:1] op_sel_hi:[0,1,1]\n\t"
          "v_pk_fma_f32 v[2:3], v[64:65], v[34:35], v[2:3] op_sel_hi:[0,1,1]\n\tv_pk_fma_f32 v[4:5], v[64:65], v[36:37], v[4:5] op_sel_hi:[0,1,1]\n\tv_pk_fma_f32 v[6:7], v[64:65], v[38:39], v[6:7] op_sel_hi:[0,1,1]\n\tv_pk_fma_f32 v[8:9], v[64:65], v[40:41], v[8:9] op_sel_hi:[0,1,1]\n\t"
          "v_pk_fma_f32 v[10:11], v[64:65], v[42:43], v[10:11] op_sel_hi:[0,1,1]\n\tv_pk_fma_f32 v[12:13], v[64:65], v[44:45], v[12:13] op_sel_hi:[0,1,1]\n\tv_pk_fma_f32 v[14:15], v[64:65], v[46:47], v[14:15] op_sel_hi:[0,1,1]\n\tv_pk_mul_f32 v[0:1], v[48:49], v[0:1]\n\t"
          "v_pk_mul_f32 v[2:3], v[50:51], v[2:3]\n\tv_pk_mul_f32 v[4:5], v[52:53], v[4:5]\n\tv_pk_mul_f32 v[6:7], v[54:55], v[6:7]\n\tv_pk_mul_f32 v[8:9], v[56:57], v[8:9]\n\t"
          "v_pk_mul_f32 v[10:11], v[58:59], v[10:11]\n\tv_pk_mul_f32 v[12:13], v[60:61], v[12:13]\n\tv_pk_mul_f32 v[14:15], v[62:63], v[14:15]\n\tv_cvt_pk_bf16_f32 v68, v0, v1\n\t"
          "v_cvt_pk_bf16_f32 v69, v2, v3\n\tv_cvt_pk_bf16_f32 v70, v4, v5\n\tv_cvt_pk_bf16_f32 v71, v6, v7\n\tv_cvt_pk_bf16_f32 v72, v8, v9\n\t"
          "v_cvt_pk_bf16_f32 v73, v10, v11\n\tv_cvt_pk_bf16_f32 v74, v12, v13\n\tv_cvt_pk_bf16_f32 v75, v14, v15\n\tbuffer_store_dwordx4 v[68:71], %[vo], %[desc], 0 offen sc1\n\t"
          "buffer_store_dwordx4 v[72:75], %[vo], %[desc], 0 offen offset:512 sc1\n\tRX%=:\n\ts_nop 1\n\t"
          : [rot] "+s"(rot), [t0] "=&s"(t0)
          : [l16] "v"(l16), [vraw] "v"(vraw), [vxa] "v"(vxa),
            [vtr] "v"(lds0 + L_SCR + (w < 4 ? (w >> 1) : 2) * 1024 + h * 3072 + q * 128 + (((w & 1) * 64 + 32 * ((lane >> 4) & 1) + 8 * (lane & 3)) ^ ((q >> 1) * 64))),
            [vr] "v"(lds0 + L_ACT + (w < 4 ? 0 : ((w >> 1) & 1) * 4096) + tok * 128 + (h ^ ((tok >> 1) & 7)) * 16),
            [vsx] "v"(lds0 + L_STAT + (lane & 7) * 512 + (8 * (w & 3) + (lane >> 3)) * 16 + (w >> 2) * 8),
            [vst] "v"(lds0 + L_STAT + tok * 16), [vtab] "v"(ltab + h * 16), [vx] "v"(ltab + tok * 4), [vo] "v"(qkv_out(OFF_L0, w, c0, j0, lane)),
            [desc] "s"(desc), [descr] "s"(dq), [descx] "s"(dx), [descj] "s"(mkdesc(src + 16 * ks0 * jld + 64 * np, 32 * jld * 2)), [jl2] "s"(jld * 2),
            [mw] "s"(mw), [mx] "s"(mx), [w] "s"(w), [unit] "s"(w < 2 ? 1024 : 2048), [sc] "s"(w < 2 ? QSCALE : 1.f)
          : "v0", "v1", "v2", "v3", "v4", "v5", "v6", "v7", "v8", "v9", "v10", "v11", "v12", "v13", "v14", "v15", "v16", "v17", "v18", "v19", "v20", "v21", "v22", "v23", "v24", "v25", "v26", "v27", "v28", "v29", "v30", "v31", "v32", "v33", "v34", "v35", "v36", "v37", "v38", "v39", "v40", "v41", "v42", "v43", "v44", "v45", "v46", "v47", "v48", "v49", "v50", "v51", "v52", "v53", "v54", "v55", "v56", "v57", "v58", "v59", "v60", "v61", "v62", "v63", "v64", "v65", "v66", "v67", "v68", "v69", "v70", "v71", "v72", "v73", "v74", "v75", "v76", "v77", "v78", "v79", "v80", "v81", "v82", "v83", "v84", "v85", "v96", "v97", "v98", "v99", "v100", "v101", "v102", "v103", "v104", "v105", "v106", "v107", "v108", "v109", "v110", "v111", "v112", "v113", "v114", "v115", "v116", "v117", "v118", "v119", "v120", "v121", "v122", "v123", "v124", "v125", "v126", "v127", "scc", "vcc", "memory");
    }
    {
      PHASE_VARS();
      if (w == 0) *(LAS u32*)(L + L_FLAG + (lane & 7) * 4) = lane >> 6;
      if (c0 == 0 && lane < 32) st4(__builtin_bit_cast(u32, tab[lane]), rs, lane * 4, OFF_CSQ + qkv_tile(w, j0) * 128);
      asm volatile("s_waitcnt vmcnt(2)" ::: "memory");
      LAS char* S = L + L_SCR + w * 3072;
      const int q = (lane & 15) >> 2;
      const int ra = (4 * h + q) * 128 + ((32 * ((lane >> 4) & 1) + 8 * (lane & 3)) ^ ((q >> 1) * 64));
#pragma unroll
      for (int i = 0; i < 4; ++i) {
        const int a = ra ^ ((i & 1) * 64);
        const s4v t0 = __builtin_amdgcn_ds_read_tr16_b64_v4i16((LAS s4v*)(S + a + 2 * 24576 + (i >> 1) * 2048));
        const s4v t1 = __builtin_amdgcn_ds_read_tr16_b64_v4i16((LAS s4v*)(S + a + ((i >> 1) ? 0 : 2 * 24576 + 1024)));
        const v2u u0 = __builtin_bit_cast(v2u, t0), u1 = __builtin_bit_cast(v2u, t1);
        st16(v4u{u0[0], u0[1], u1[0], u1[1]}, rs, l16, jd[i]);
      }
      publish(rs, flag_off(SL_QKV, j0 * 32 + c0), tag, w, lane);
      const rsrc_t rx = __builtin_amdgcn_make_buffer_rsrc((void*)xin, 0, 2048 * 1024, 0x00020000);
      const u32 vr_ = (64 * c + 32 * (w & 1) + tok) * 1024 + 128 * j + 32 * (w >> 1) + 8 * h;
      const v2u xr0 = __builtin_amdgcn_raw_buffer_load_b64(rx, vr_, 0, 0), xr1 = __builtin_amdgcn_raw_buffer_load_b64(rx, vr_ + 16, 0, 0);
      const u32 bv1 = __builtin_amdgcn_raw_buffer_load_b16(__builtin_amdgcn_make_buffer_rsrc((void*)b1, 0, FF * 2, 0x00020000), 64 * (7 * j + (w < 6 ? w : 6)) + 2 * tok, 0, 0);
      const u32 bv2 = __builtin_amdgcn_raw_buffer_load_b16(__builtin_amdgcn_make_buffer_rsrc((void*)b2, 0, DM * 2, 0x00020000), 128 * j + 32 * (w >> 1) + 2 * (lane & 15), 0, 0);
      *(LAS v4u*)(L + L_XRES + w * 1024 + l16) = v4u{xr0[0], xr0[1], xr1[0], xr1[1]};
      tab[64 + tok] = GELU_TH * __builtin_bit_cast(float, bv1 << 16);
      tab[160 + (lane & 15)] = __builtin_bit_cast(float, bv2 << 16);
    }
  }

#pragma unroll 1
  for (int layer = 0; layer < 2; ++layer) {
    const u32 LB = OFF_L0 + layer * L_STRIDE;
    const int sl = 5 * layer;
    {
      PHASE_VARS();
      const int nt1 = 7 * j + (w < 6 ? w : 6);
      const int head = 4 * xc + (w & 3), half = w >> 2;
      const u32 oso = OFF_L0 + LO_O + ((2 * bi + half) * 32 + head) * 1024, osf = flag_off(SL_O + sl, bi * 8 + xc);
      u32 bad;
      {
        u32 skv = LB + LO_KV + (head * 64 + half * 32) * 2048, sw1 = OFF_W1 + nt1 * 32768, swo = OFF_WO + j * 65536 + w * 8192;
        u32 swm = OFF_W2 + (bi * 8 + w) * 7 * 1024;
        const int pa = 48 + (nt1 & ~1), pb = 104 + 2 * j, pc = qkv_tile(w, j) & ~1;
        const int pf = (lane & 8) ? ((lane & 4) ? pc : (lane & 2) ? pb : pa) + (lane & 1) : 120 + 8 * j + (lane & 7);
        asm volatile(
          "s_nop 4\n\tv_mov_b32 v92, %[vin]\n\tv_mov_b32 v93, %[vin]\n\tv_mov_b32 v94, %[vin]\n\t"
          "v_mov_b32 v95, %[vin]\n\tv_mov_b32 v96, %[vin]\n\tv_mov_b32 v97, %[vin]\n\tv_mov_b32 v98, %[vin]\n\t"
          "v_mov_b32 v99, %[vin]\n\tv_mov_b32 v104, %[vin]\n\tv_mov_b32 v105, %[vin]\n\tv_mov_b32 v106, %[vin]\n\t"
          "v_mov_b32 v107, %[vin]\n\tv_mov_b32 v108, %[vin]\n\tv_mov_b32 v109, %[vin]\n\tv_mov_b32 v110, %[vin]\n\t"
          "v_mov_b32 v111, %[vin]\n\tv_mov_b32 v116, %[vin]\n\tv_mov_b32 v117, %[vin]\n\tv_mov_b32 v118, %[vin]\n\t"
          "v_mov_b32 v119, %[vin]\n\tv_mov_b32 v120, %[vin]\n\tv_mov_b32 v121, %[vin]\n\tv_mov_b32 v122, %[vin]\n\t"
          "v_mov_b32 v123, %[vin]\n\tv_mov_b32 v0, 0\n\tv_mov_b32 v1, 0\n\tv_mov_b32 v2, 0\n\t"
          "v_mov_b32 v3, 0\n\tv_mov_b32 v4, 0\n\tv_mov_b32 v5, 0\n\tv_mov_b32 v6, 0\n\t"
          "v_mov_b32 v7, 0\n\tv_mov_b32 v8, 0\n\tv_mov_b32 v9, 0\n\tv_mov_b32 v10, 0\n\t"
          "v_mov_b32 v11, 0\n\tv_mov_b32 v12, 0\n\tv_mov_b32 v13, 0\n\tv_mov_b32 v14, 0\n\t"
          "v_mov_b32 v15, 0\n\tv_mov_b32 v16, 0\n\tv_mov_b32 v17, 0\n\tv_mov_b32 v18, 0\n\t"
          "v_mov_b32 v19, 0\n\tv_mov_b32 v20, 0\n\tv_mov_b32 v21, 0\n\tv_mov_b32 v22, 0\n\t"
          "v_mov_b32 v23, 0\n\tv_mov_b32 v24, 0\n\tv_mov_b32 v25, 0\n\tv_mov_b32 v26, 0\n\t"
          "v_mov_b32 v27, 0\n\tv_mov_b32 v28, 0\n\tv_mov_b32 v29, 0\n\tv_mov_b32 v30, 0\n\t"
          "v_mov_b32 v31, 0\n\ts_cmp_lg_u32 %[w], 0\n\ts_cbranch_scc1 AW%=\n\tAQA%=:\n\t"
          "buffer_load_dwordx2 v[124:125], %[vf], %[desc], 0 offen sc1\n\ts_waitcnt vmcnt(0)\n\tv_cmp_ne_u64 vcc, %[tag], v[124:125]\n\ts_cbranch_vccz AQB%=\n\t"
          "s_sub_u32 %[bud], %[bud], 1\n\ts_cbranch_scc0 AQA%=\n\ts_mov_b32 %[bud], 0\n\tAQB%=:\n\t"
          "AW%=:\n\ts_barrier\n\tbuffer_load_dwordx4 v[48:51], %[l16], %[desc], %[sq] offen sc1\n\tbuffer_load_dwordx4 v[88:91], %[l16], %[desc], %[skv] offen offset:0 sc1\n\t"
          "buffer_load_dwordx4 v[52:55], %[l16], %[desc], %[sq] offen offset:1024 sc1\n\ts_mov_b64 exec, %[msk]\n\tbuffer_load_dwordx4 v[92:95], %[vov], %[desc], %[skv] offen offset:1024 sc1\n\ts_mov_b64 exec, -1\n\t"
          "s_mov_b64 exec, %[msk]\n\tbuffer_load_dwordx4 v[96:99], %[vov], %[desc], %[skv] offen offset:1536 sc1\n\ts_mov_b64 exec, -1\n\tbuffer_load_dwordx4 v[100:103], %[l16], %[desc], %[skv] offen offset:2048 sc1\n\t"
          "s_mov_b64 exec, %[msk]\n\tbuffer_load_dwordx4 v[104:107], %[vov], %[desc], %[skv] offen offset:3072 sc1\n\ts_mov_b64 exec, -1\n\ts_mov_b64 exec, %[msk]\n\t"
          "buffer_load_dwordx4 v[108:111], %[vov], %[desc], %[skv] offen offset:3584 sc1\n\ts_mov_b64 exec, -1\n\ts_add_u32 %[skv], %[skv], 4096\n\tbuffer_load_dwordx4 v[112:115], %[l16], %[desc], %[skv] offen offset:0 sc1\n\t"
          "s_mov_b64 exec, %[msk]\n\tbuffer_load_dwordx4 v[116:119], %[vov], %[desc], %[skv] offen offset:1024 sc1\n\ts_mov_b64 exec, -1\n\ts_cmp_lt_u32 %[w], 4\n\t"
          "s_cbranch_scc1 AP%=\n\ts_setprio 1\n\tAP%=:\n\ts_waitcnt vmcnt(8)\n\t"
          "v_mfma_f32_32x32x16_bf16 v[56:71], v[88:91], v[48:51], 0\n\ts_waitcnt vmcnt(7)\n\ts_nop 12\n\tv_exp_f32 v56, v56\n\t"
          "v_exp_f32 v57, v57\n\tv_exp_f32 v58, v58\n\tv_exp_f32 v59, v59\n\ts_mov_b64 exec, %[msk]\n\t"
          "buffer_load_dwordx4 v[120:123], %[vov], %[desc], %[skv] offen offset:1536 sc1\n\ts_mov_b64 exec, -1\n\tv_exp_f32 v60, v60\n\tv_mfma_f32_32x32x16_bf16 v[72:87], v[88:91], v[52:55], 0\n\t"
          "v_exp_f32 v61, v61\n\tv_exp_f32 v62, v62\n\tbuffer_load_dwordx4 v[88:91], %[l16], %[desc], %[skv] offen offset:2048 sc1\n\tv_exp_f32 v63, v63\n\t"
          "v_exp_f32 v64, v64\n\tv_exp_f32 v65, v65\n\ts_waitcnt vmcnt(8)\n\tv_smfmac_f32_32x32x32_bf16 v[0:15], v[92:95], v[56:63], %[idx]\n\t"
          "v_exp_f32 v66, v66\n\tv_exp_f32 v67, v67\n\tv_exp_f32 v68, v68\n\tv_exp_f32 v69, v69\n\t"
          "v_exp_f32 v70, v70\n\tv_exp_f32 v71, v71\n\tv_exp_f32 v72, v72\n\tv_exp_f32 v73, v73\n\t"
          "s_waitcnt vmcnt(7)\n\tv_smfmac_f32_32x32x32_bf16 v[0:15], v[96:99], v[64:71], %[idx]\n\tv_exp_f32 v74, v74\n\tv_exp_f32 v75, v75\n\t"
          "v_exp_f32 v76, v76\n\ts_waitcnt vmcnt(6)\n\tv_mfma_f32_32x32x16_bf16 v[56:71], v[100:103], v[48:51], 0\n\tv_exp_f32 v77, v77\n\t"
          "v_exp_f32 v78, v78\n\tv_exp_f32 v79, v79\n\tv_exp_f32 v80, v80\n\tv_exp_f32 v81, v81\n\t"
          "v_smfmac_f32_32x32x32_bf16 v[16:31], v[92:95], v[72:79], %[idx]\n\tv_exp_f32 v82, v82\n\tv_exp_f32 v83, v83\n\ts_mov_b64 exec, %[msk]\n\t"
          "buffer_load_dwordx4 v[92:95], %[vov], %[desc], %[skv] offen offset:3072 sc1\n\ts_mov_b64 exec, -1\n\tv_exp_f32 v84, v84\n\tv_exp_f32 v85, v85\n\t"
          "v_exp_f32 v86, v86\n\tv_exp_f32 v87, v87\n\tv_exp_f32 v56, v56\n\tv_exp_f32 v57, v57\n\t"
          "v_smfmac_f32_32x32x32_bf16 v[16:31], v[96:99], v[80:87], %[idx]\n\tv_exp_f32 v58, v58\n\tv_exp_f32 v59, v59\n\ts_mov_b64 exec, %[msk]\n\t"
          "buffer_load_dwordx4 v[96:99], %[vov], %[desc], %[skv] offen offset:3584 sc1\n\ts_mov_b64 exec, -1\n\ts_add_u32 %[skv], %[skv], 4096\n\tv_exp_f32 v60, v60\n\t"
          "v_mfma_f32_32x32x16_bf16 v[72:87], v[100:103], v[52:55], 0\n\tv_exp_f32 v61, v61\n\tv_exp_f32 v62, v62\n\tbuffer_load_dwordx4 v[100:103], %[l16], %[desc], %[skv] offen offset:0 sc1\n\t"
          "v_exp_f32 v63, v63\n\tv_exp_f32 v64, v64\n\tv_exp_f32 v65, v65\n\ts_waitcnt vmcnt(8)\n\t"
          "v_smfmac_f32_32x32x32_bf16 v[0:15], v[104:107], v[56:63], %[idx]\n\tv_exp_f32 v66, v66\n\tv_exp_f32 v67, v67\n\tv_exp_f32 v68, v68\n\t"
          "v_exp_f32 v69, v69\n\tv_exp_f32 v70, v70\n\tv_exp_f32 v71, v71\n\tv_exp_f32 v72, v72\n\t"
          "v_exp_f32 v73, v73\n\ts_waitcnt vmcnt(7)\n\tv_smfmac_f32_32x32x32_bf16 v[0:15], v[108:111], v[64:71], %[idx]\n\tv_exp_f32 v74, v74\n\t"
          "v_exp_f32 v75, v75\n\tv_exp_f32 v76, v76\n\ts_waitcnt vmcnt(6)\n\tv_mfma_f32_32x32x16_bf16 v[56:71], v[112:115], v[48:51], 0\n\t"
          "v_exp_f32 v77, v77\n\tv_exp_f32 v78, v78\n\tv_exp_f32 v79, v79\n\tv_exp_f32 v80, v80\n\t"
          "v_exp_f32 v81, v81\n\tv_smfmac_f32_32x32x32_bf16 v[16:31], v[104:107], v[72:79], %[idx]\n\tv_exp_f32 v82, v82\n\tv_exp_f32 v83, v83\n\t"
          "s_mov_b64 exec, %[msk]\n\tbuffer_load_dwordx4 v[104:107], %[vov], %[desc], %[skv] offen offset:1024 sc1\n\ts_mov_b64 exec, -1\n\tv_exp_f32 v84, v84\n\t"
          "v_exp_f32 v85, v85\n\tv_exp_f32 v86, v86\n\tv_exp_f32 v87, v87\n\tv_exp_f32 v56, v56\n\t"
          "v_exp_f32 v57, v57\n\tv_smfmac_f32_32x32x32_bf16 v[16:31], v[108:111], v[80:87], %[idx]\n\tv_exp_f32 v58, v58\n\tv_exp_f32 v59, v59\n\t"
          "s_mov_b64 exec, %[msk]\n\tbuffer_load_dwordx4 v[108:111], %[vov], %[desc], %[skv] offen offset:1536 sc1\n\ts_mov_b64 exec, -1\n\tv_exp_f32 v60, v60\n\t"
          "v_mfma_f32_32x32x16_bf16 v[72:87], v[112:115], v[52:55], 0\n\tv_exp_f32 v61, v61\n\tv_exp_f32 v62, v62\n\tbuffer_load_dwordx4 v[112:115], %[l16], %[desc], %[skv] offen offset:2048 sc1\n\t"
          "v_exp_f32 v63, v63\n\tv_exp_f32 v64, v64\n\tv_exp_f32 v65, v65\n\ts_waitcnt vmcnt(8)\n\t"
          "v_smfmac_f32_32x32x32_bf16 v[0:15], v[116:119], v[56:63], %[idx]\n\tv_exp_f32 v66, v66\n\tv_exp_f32 v67, v67\n\tv_exp_f32 v68, v68\n\t"
          "v_exp_f32 v69, v69\n\tv_exp_f32 v70, v70\n\tbuffer_load_dwordx2 v[124:125], %[vpf], %[desc], 0 offen sc1\n\tv_exp_f32 v71, v71\n\t"
          "v_exp_f32 v72, v72\n\tv_exp_f32 v73, v73\n\ts_waitcnt vmcnt(8)\n\tv_smfmac_f32_32x32x32_bf16 v[0:15], v[120:123], v[64:71], %[idx]\n\t"
          "v_exp_f32 v74, v74\n\tv_exp_f32 v75, v75\n\tv_exp_f32 v76, v76\n\ts_waitcnt vmcnt(7)\n\t"
          "v_mfma_f32_32x32x16_bf16 v[56:71], v[88:91], v[48:51], 0\n\tv_exp_f32 v77, v77\n\tv_exp_f32 v78, v78\n\tv_exp_f32 v79, v79\n\t"
          "v_exp_f32 v80, v80\n\tv_exp_f32 v81, v81\n\tv_smfmac_f32_32x32x32_bf16 v[16:31], v[116:119], v[72:79], %[idx]\n\tv_exp_f32 v82, v82\n\t"
          "v_exp_f32 v83, v83\n\ts_mov_b64 exec, %[msk]\n\tbuffer_load_dwordx4 v[116:119], %[vov], %[desc], %[skv] offen offset:3072 sc1\n\ts_mov_b64 exec, -1\n\t"
          "v_exp_f32 v84, v84\n\tv_exp_f32 v85, v85\n\tv_exp_f32 v86, v86\n\tv_exp_f32 v87, v87\n\t"
          "v_exp_f32 v56, v56\n\tv_exp_f32 v57, v57\n\tv_smfmac_f32_32x32x32_bf16 v[16:31], v[120:123], v[80:87], %[idx]\n\tv_exp_f32 v58, v58\n\t"
          "v_exp_f32 v59, v59\n\ts_mov_b64 exec, %[msk]\n\tbuffer_load_dwordx4 v[120:123], %[vov], %[desc], %[skv] offen offset:3584 sc1\n\ts_mov_b64 exec, -1\n\t"
          "s_add_u32 %[skv], %[skv], 4096\n\tv_exp_f32 v60, v60\n\tv_mfma_f32_32x32x16_bf16 v[72:87], v[88:91], v[52:55], 0\n\tv_exp_f32 v61, v61\n\t"
          "v_exp_f32 v62, v62\n\tbuffer_load_dwordx4 v[88:91], %[l16], %[desc], %[skv] offen offset:0 sc1\n\tv_exp_f32 v63, v63\n\tv_exp_f32 v64, v64\n\t"
          "v_exp_f32 v65, v65\n\ts_waitcnt vmcnt(9)\n\tv_smfmac_f32_32x32x32_bf16 v[0:15], v[92:95], v[56:63], %[idx]\n\tv_exp_f32 v66, v66\n\t"
          "v_exp_f32 v67, v67\n\tv_exp_f32 v68, v68\n\tv_exp_f32 v69, v69\n\tv_exp_f32 v70, v70\n\t"
          "v_exp_f32 v71, v71\n\tv_exp_f32 v72, v72\n\tv_exp_f32 v73, v73\n\ts_waitcnt vmcnt(8)\n\t"
          "v_smfmac_f32_32x32x32_bf16 v[0:15], v[96:99], v[64:71], %[idx]\n\tv_exp_f32 v74, v74\n\tv_exp_f32 v75, v75\n\tv_exp_f32 v76, v76\n\t"
          "s_waitcnt vmcnt(7)\n\tv_mfma_f32_32x32x16_bf16 v[56:71], v[100:103], v[48:51], 0\n\tv_exp_f32 v77, v77\n\tv_exp_f32 v78, v78\n\t"
          "v_exp_f32 v79, v79\n\tv_exp_f32 v80, v80\n\tv_exp_f32 v81, v81\n\tv_smfmac_f32_32x32x32_bf16 v[16:31], v[92:95], v[72:79], %[idx]\n\t"
          "v_exp_f32 v82, v82\n\tv_exp_f32 v83, v83\n\ts_mov_b64 exec, %[msk]\n\tbuffer_load_dwordx4 v[92:95], %[vov], %[desc], %[skv] offen offset:1024 sc1\n\t"
          "s_mov_b64 exec, -1\n\tv_exp_f32 v84, v84\n\tv_exp_f32 v85, v85\n\tv_exp_f32 v86, v86\n\t"
          "v_exp_f32 v87, v87\n\tv_exp_f32 v56, v56\n\tv_exp_f32 v57, v57\n\tv_smfmac_f32_32x32x32_bf16 v[16:31], v[96:99], v[80:87], %[idx]\n\t"
          "v_exp_f32 v58, v58\n\tv_exp_f32 v59, v59\n\ts_mov_b64 exec, %[msk]\n\tbuffer_load_dwordx4 v[96:99], %[vov], %[desc], %[skv] offen offset:1536 sc1\n\t"
          "s_mov_b64 exec, -1\n\tv_exp_f32 v60, v60\n\tv_mfma_f32_32x32x16_bf16 v[72:87], v[100:103], v[52:55], 0\n\tv_exp_f32 v61, v61\n\t"
          "v_exp_f32 v62, v62\n\tbuffer_load_dwordx4 v[100:103], %[l16], %[desc], %[skv] offen offset:2048 sc1\n\tv_exp_f32 v63, v63\n\tv_exp_f32 v64, v64\n\t"
          "v_exp_f32 v65, v65\n\ts_waitcnt vmcnt(9)\n\tv_smfmac_f32_32x32x32_bf16 v[0:15], v[104:107], v[56:63], %[idx]\n\tv_exp_f32 v66, v66\n\t"
          "v_exp_f32 v67, v67\n\tv_exp_f32 v68, v68\n\tv_exp_f32 v69, v69\n\tv_exp_f32 v70, v70\n\t"
          "s_waitcnt vmcnt(6)\n\tv_cmp_ne_u64 vcc, %[tag], v[124:125]\n\ts_cbranch_vccz AFB%=\n\tAFA%=:\n\t"
          "buffer_load_dwordx2 v[124:125], %[vpf], %[desc], 0 offen sc1\n\ts_waitcnt vmcnt(0)\n\tv_cmp_ne_u64 vcc, %[tag], v[124:125]\n\ts_cbranch_vccz AFB%=\n\t"
          "s_sub_u32 %[bud], %[bud], 1\n\ts_cbranch_scc0 AFA%=\n\ts_mov_b32 %[bud], 0\n\tAFB%=:\n\t"
          "v_exp_f32 v71, v71\n\tv_exp_f32 v72, v72\n\tv_exp_f32 v73, v73\n\tv_smfmac_f32_32x32x32_bf16 v[0:15], v[108:111], v[64:71], %[idx]\n\t"
          "v_exp_f32 v74, v74\n\tv_exp_f32 v75, v75\n\tv_exp_f32 v76, v76\n\tv_mfma_f32_32x32x16_bf16 v[56:71], v[112:115], v[48:51], 0\n\t"
          "v_exp_f32 v77, v77\n\tv_exp_f32 v78, v78\n\tv_exp_f32 v79, v79\n\tv_exp_f32 v80, v80\n\t"
          "v_exp_f32 v81, v81\n\tv_smfmac_f32_32x32x32_bf16 v[16:31], v[104:107], v[72:79], %[idx]\n\tv_exp_f32 v82, v82\n\tv_exp_f32 v83, v83\n\t"
          "s_mov_b64 exec, %[msk]\n\tbuffer_load_dwordx4 v[104:107], %[vov], %[desc], %[skv] offen offset:3072 sc1\n\ts_mov_b64 exec, -1\n\tv_exp_f32 v84, v84\n\t"
          "v_exp_f32 v85, v85\n\tv_exp_f32 v86, v86\n\tv_exp_f32 v87, v87\n\tv_exp_f32 v56, v56\n\t"
          "v_exp_f32 v57, v57\n\tv_smfmac_f32_32x32x32_bf16 v[16:31], v[108:111], v[80:87], %[idx]\n\tv_exp_f32 v58, v58\n\tv_exp_f32 v59, v59\n\t"
          "s_mov_b64 exec, %[msk]\n\tbuffer_load_dwordx4 v[108:111], %[vov], %[desc], %[skv] offen offset:3584 sc1\n\ts_mov_b64 exec, -1\n\ts_add_u32 %[skv], %[skv], 4096\n\t"
          "v_exp_f32 v60, v60\n\tv_mfma_f32_32x32x16_bf16 v[72:87], v[112:115], v[52:55], 0\n\tv_exp_f32 v61, v61\n\tv_exp_f32 v62, v62\n\t"
          "buffer_load_dwordx4 v[112:115], %[l16], %[desc], %[skv] offen offset:0 sc1\n\tv_exp_f32 v63, v63\n\tv_exp_f32 v64, v64\n\tv_exp_f32 v65, v65\n\t"
          "s_waitcnt vmcnt(8)\n\tv_smfmac_f32_32x32x32_bf16 v[0:15], v[116:119], v[56:63], %[idx]\n\tv_exp_f32 v66, v66\n\tv_exp_f32 v67, v67\n\t"
          "v_exp_f32 v68, v68\n\tbuffer_load_dwordx4 v[128:131], %[l16], %[desc], %[sw1] offen offset:0 sc1\n\tv_exp_f32 v69, v69\n\tv_exp_f32 v70, v70\n\t"
          "v_exp_f32 v71, v71\n\tv_exp_f32 v72, v72\n\tv_exp_f32 v73, v73\n\ts_waitcnt vmcnt(8)\n\t"
          "v_smfmac_f32_32x32x32_bf16 v[0:15], v[120:123], v[64:71], %[idx]\n\tv_exp_f32 v74, v74\n\tv_exp_f32 v75, v75\n\tv_exp_f32 v76, v76\n\t"
          "s_waitcnt vmcnt(7)\n\tv_mfma_f32_32x32x16_bf16 v[56:71], v[88:91], v[48:51], 0\n\tv_exp_f32 v77, v77\n\tv_exp_f32 v78, v78\n\t"
          "v_exp_f32 v79, v79\n\tv_exp_f32 v80, v80\n\tv_exp_f32 v81, v81\n\tv_smfmac_f32_32x32x32_bf16 v[16:31], v[116:119], v[72:79], %[idx]\n\t"
          "v_exp_f32 v82, v82\n\tv_exp_f32 v83, v83\n\ts_mov_b64 exec, %[msk]\n\tbuffer_load_dwordx4 v[116:119], %[vov], %[desc], %[skv] offen offset:1024 sc1\n\t"
          "s_mov_b64 exec, -1\n\tv_exp_f32 v84, v84\n\tv_exp_f32 v85, v85\n\tbuffer_load_dwordx4 v[132:135], %[l16], %[desc], %[sw1] offen offset:1024 sc1\n\t"
          "v_exp_f32 v86, v86\n\ts_mov_b32 m0, %[mwo]\n\ts_nop 0\n\tbuffer_load_dwordx4 %[l16], %[desc], %[swo] offen offset:0 sc1 lds\n\t"
          "v_exp_f32 v87, v87\n\tv_exp_f32 v56, v56\n\tv_exp_f32 v57, v57\n\tv_smfmac_f32_32x32x32_bf16 v[16:31], v[120:123], v[80:87], %[idx]\n\t"
          "v_exp_f32 v58, v58\n\tv_exp_f32 v59, v59\n\ts_mov_b64 exec, %[msk]\n\tbuffer_load_dwordx4 v[120:123], %[vov], %[desc], %[skv] offen offset:1536 sc1\n\t"
          "s_mov_b64 exec, -1\n\tv_exp_f32 v60, v60\n\tv_mfma_f32_32x32x16_bf16 v[72:87], v[88:91], v[52:55], 0\n\tv_exp_f32 v61, v61\n\t"
          "v_exp_f32 v62, v62\n\tbuffer_load_dwordx4 v[88:91], %[l16], %[desc], %[skv] offen offset:2048 sc1\n\tv_exp_f32 v63, v63\n\tv_exp_f32 v64, v64\n\t"
          "v_exp_f32 v65, v65\n\ts_waitcnt vmcnt(11)\n\tv_smfmac_f32_32x32x32_bf16 v[0:15], v[92:95], v[56:63], %[idx]\n\tv_exp_f32 v66, v66\n\t"
          "v_exp_f32 v67, v67\n\tv_exp_f32 v68, v68\n\tbuffer_load_dwordx4 v[136:139], %[l16], %[desc], %[sw1] offen offset:2048 sc1\n\tv_exp_f32 v69, v69\n\t"
          "v_exp_f32 v70, v70\n\tv_exp_f32 v71, v71\n\tv_exp_f32 v72, v72\n\tv_exp_f32 v73, v73\n\t"
          "s_waitcnt vmcnt(11)\n\tv_smfmac_f32_32x32x32_bf16 v[0:15], v[96:99], v[64:71], %[idx]\n\tv_exp_f32 v74, v74\n\tv_exp_f32 v75, v75\n\t"
          "v_exp_f32 v76, v76\n\ts_waitcnt vmcnt(10)\n\tv_mfma_f32_32x32x16_bf16 v[56:71], v[100:103], v[48:51], 0\n\tv_exp_f32 v77, v77\n\t"
          "v_exp_f32 v78, v78\n\tv_exp_f32 v79, v79\n\tv_exp_f32 v80, v80\n\tv_exp_f32 v81, v81\n\t"
          "v_smfmac_f32_32x32x32_bf16 v[16:31], v[92:95], v[72:79], %[idx]\n\tv_exp_f32 v82, v82\n\tv_exp_f32 v83, v83\n\ts_mov_b64 exec, %[msk]\n\t"
          "buffer_load_dwordx4 v[92:95], %[vov], %[desc], %[skv] offen offset:3072 sc1\n\ts_mov_b64 exec, -1\n\tv_exp_f32 v84, v84\n\tv_exp_f32 v85, v85\n\t"
          "v_exp_f32 v86, v86\n\tv_exp_f32 v87, v87\n\tv_exp_f32 v56, v56\n\tv_exp_f32 v57, v57\n\t"
          "v_smfmac_f32_32x32x32_bf16 v[16:31], v[96:99], v[80:87], %[idx]\n\tv_exp_f32 v58, v58\n\tv_exp_f32 v59, v59\n\ts_mov_b64 exec, %[msk]\n\t"
          "buffer_load_dwordx4 v[96:99], %[vov], %[desc], %[skv] offen offset:3584 sc1\n\ts_mov_b64 exec, -1\n\ts_add_u32 %[skv], %[skv], 4096\n\tv_exp_f32 v60, v60\n\t"
          "v_mfma_f32_32x32x16_bf16 v[72:87], v[100:103], v[52:55], 0\n\tv_exp_f32 v61, v61\n\tv_exp_f32 v62, v62\n\tbuffer_load_dwordx4 v[100:103], %[l16], %[desc], %[skv] offen offset:0 sc1\n\t"
          "v_exp_f32 v63, v63\n\tv_exp_f32 v64, v64\n\tv_exp_f32 v65, v65\n\ts_waitcnt vmcnt(12)\n\t"
          "v_smfmac_f32_32x32x32_bf16 v[0:15], v[104:107], v[56:63], %[idx]\n\tv_exp_f32 v66, v66\n\tv_exp_f32 v67, v67\n\tv_exp_f32 v68, v68\n\t"
          "buffer_load_dwordx4 v[140:143], %[l16], %[desc], %[sw1] offen offset:3072 sc1\n\ts_add_u32 %[sw1], %[sw1], 4096\n\tv_exp_f32 v69, v69\n\tv_exp_f32 v70, v70\n\t"
          "v_exp_f32 v71, v71\n\tv_exp_f32 v72, v72\n\tv_exp_f32 v73, v73\n\ts_waitcnt vmcnt(12)\n\t"
          "v_smfmac_f32_32x32x32_bf16 v[0:15], v[108:111], v[64:71], %[idx]\n\tv_exp_f32 v74, v74\n\tv_exp_f32 v75, v75\n\tv_exp_f32 v76, v76\n\t"
          "s_waitcnt vmcnt(11)\n\tv_mfma_f32_32x32x16_bf16 v[56:71], v[112:115], v[48:51], 0\n\tv_exp_f32 v77, v77\n\tv_exp_f32 v78, v78\n\t"
          "v_exp_f32 v79, v79\n\tv_exp_f32 v80, v80\n\tv_exp_f32 v81, v81\n\tv_smfmac_f32_32x32x32_bf16 v[16:31], v[104:107], v[72:79], %[idx]\n\t"
          "v_exp_f32 v82, v82\n\tv_exp_f32 v83, v83\n\ts_mov_b64 exec, %[msk]\n\tbuffer_load_dwordx4 v[104:107], %[vov], %[desc], %[skv] offen offset:1024 sc1\n\t"
          "s_mov_b64 exec, -1\n\tv_exp_f32 v84, v84\n\tv_exp_f32 v85, v85\n\tbuffer_load_dwordx4 v[144:147], %[l16], %[desc], %[sw1] offen offset:0 sc1\n\t"
          "v_exp_f32 v86, v86\n\tv_exp_f32 v87, v87\n\tv_exp_f32 v56, v56\n\tv_exp_f32 v57, v57\n\t"
          "v_smfmac_f32_32x32x32_bf16 v[16:31], v[108:111], v[80:87], %[idx]\n\tv_exp_f32 v58, v58\n\tv_exp_f32 v59, v59\n\ts_mov_b64 exec, %[msk]\n\t"
          "buffer_load_dwordx4 v[108:111], %[vov], %[desc], %[skv] offen offset:1536 sc1\n\ts_mov_b64 exec, -1\n\tv_exp_f32 v60, v60\n\tv_mfma_f32_32x32x16_bf16 v[72:87], v[112:115], v[52:55], 0\n\t"
          "v_exp_f32 v61, v61\n\tv_exp_f32 v62, v62\n\tbuffer_load_dwordx4 v[112:115], %[l16], %[desc], %[skv] offen offset:2048 sc1\n\tv_exp_f32 v63, v63\n\t"
          "v_exp_f32 v64, v64\n\tv_exp_f32 v65, v65\n\ts_waitcnt vmcnt(13)\n\tv_smfmac_f32_32x32x32_bf16 v[0:15], v[116:119], v[56:63], %[idx]\n\t"
          "v_exp_f32 v66, v66\n\tv_exp_f32 v67, v67\n\tv_exp_f32 v68, v68\n\tv_exp_f32 v69, v69\n\t"
          "v_exp_f32 v70, v70\n\tv_exp_f32 v71, v71\n\tv_exp_f32 v72, v72\n\tv_exp_f32 v73, v73\n\t"
          "s_waitcnt vmcnt(10)\n\tv_smfmac_f32_32x32x32_bf16 v[0:15], v[120:123], v[64:71], %[idx]\n\tv_exp_f32 v74, v74\n\tv_exp_f32 v75, v75\n\t"
          "v_exp_f32 v76, v76\n\ts_waitcnt vmcnt(9)\n\tv_mfma_f32_32x32x16_bf16 v[56:71], v[88:91], v[48:51], 0\n\tv_exp_f32 v77, v77\n\t"
          "v_exp_f32 v78, v78\n\tv_exp_f32 v79, v79\n\tv_exp_f32 v80, v80\n\tv_exp_f32 v81, v81\n\t"
          "v_smfmac_f32_32x32x32_bf16 v[16:31], v[116:119], v[72:79], %[idx]\n\tv_exp_f32 v82, v82\n\tv_exp_f32 v83, v83\n\ts_mov_b64 exec, %[msk]\n\t"
          "buffer_load_dwordx4 v[116:119], %[vov], %[desc], %[skv] offen offset:3072 sc1\n\ts_mov_b64 exec, -1\n\tv_exp_f32 v84, v84\n\tv_exp_f32 v85, v85\n\t"
          "buffer_load_dwordx4 v[148:151], %[l16], %[desc], %[sw1] offen offset:1024 sc1\n\tv_exp_f32 v86, v86\n\tbuffer_load_dwordx4 %[l16], %[desc], %[swo] offen offset:1024 sc1 lds\n\tv_exp_f32 v87, v87\n\t"
          "v_exp_f32 v56, v56\n\tv_exp_f32 v57, v57\n\tv_smfmac_f32_32x32x32_bf16 v[16:31], v[120:123], v[80:87], %[idx]\n\tv_exp_f32 v58, v58\n\t"
          "v_exp_f32 v59, v59\n\ts_mov_b64 exec, %[msk]\n\tbuffer_load_dwordx4 v[120:123], %[vov], %[desc], %[skv] offen offset:3584 sc1\n\ts_mov_b64 exec, -1\n\t"
          "s_add_u32 %[skv], %[skv], 4096\n\tv_exp_f32 v60, v60\n\tv_mfma_f32_32x32x16_bf16 v[72:87], v[88:91], v[52:55], 0\n\tv_exp_f32 v61, v61\n\t"
          "v_exp_f32 v62, v62\n\tbuffer_load_dwordx4 v[88:91], %[l16], %[desc], %[skv] offen offset:0 sc1\n\tv_exp_f32 v63, v63\n\tv_exp_f32 v64, v64\n\t"
          "v_exp_f32 v65, v65\n\ts_waitcnt vmcnt(12)\n\tv_smfmac_f32_32x32x32_bf16 v[0:15], v[92:95], v[56:63], %[idx]\n\tv_exp_f32 v66, v66\n\t"
          "v_exp_f32 v67, v67\n\tv_exp_f32 v68, v68\n\tbuffer_load_dwordx4 v[152:155], %[l16], %[desc], %[sw1] offen offset:2048 sc1\n\tv_exp_f32 v69, v69\n\t"
          "v_exp_f32 v70, v70\n\tv_exp_f32 v71, v71\n\tv_exp_f32 v72, v72\n\tv_exp_f32 v73, v73\n\t"
          "s_waitcnt vmcnt(12)\n\tv_smfmac_f32_32x32x32_bf16 v[0:15], v[96:99], v[64:71], %[idx]\n\tv_exp_f32 v74, v74\n\tv_exp_f32 v75, v75\n\t"
          "v_exp_f32 v76, v76\n\ts_waitcnt vmcnt(11)\n\tv_mfma_f32_32x32x16_bf16 v[56:71], v[100:103], v[48:51], 0\n\tv_exp_f32 v77, v77\n\t"
          "v_exp_f32 v78, v78\n\tv_exp_f32 v79, v79\n\tv_exp_f32 v80, v80\n\tv_exp_f32 v81, v81\n\t"
          "v_smfmac_f32_32x32x32_bf16 v[16:31], v[92:95], v[72:79], %[idx]\n\tv_exp_f32 v82, v82\n\tv_exp_f32 v83, v83\n\ts_mov_b64 exec, %[msk]\n\t"
          "buffer_load_dwordx4 v[92:95], %[vov], %[desc], %[skv] offen offset:1024 sc1\n\ts_mov_b64 exec, -1\n\tv_exp_f32 v84, v84\n\tv_exp_f32 v85, v85\n\t"
          "v_exp_f32 v86, v86\n\tv_exp_f32 v87, v87\n\tv_exp_f32 v56, v56\n\tv_exp_f32 v57, v57\n\t"
          "v_smfmac_f32_32x32x32_bf16 v[16:31], v[96:99], v[80:87], %[idx]\n\tv_exp_f32 v58, v58\n\tv_exp_f32 v59, v59\n\ts_mov_b64 exec, %[msk]\n\t"
          "buffer_load_dwordx4 v[96:99], %[vov], %[desc], %[skv] offen offset:1536 sc1\n\ts_mov_b64 exec, -1\n\tv_exp_f32 v60, v60\n\tv_mfma_f32_32x32x16_bf16 v[72:87], v[100:103], v[52:55], 0\n\t"
          "v_exp_f32 v61, v61\n\tv_exp_f32 v62, v62\n\tbuffer_load_dwordx4 v[100:103], %[l16], %[desc], %[skv] offen offset:2048 sc1\n\tv_exp_f32 v63, v63\n\t"
          "v_exp_f32 v64, v64\n\tv_exp_f32 v65, v65\n\ts_waitcnt vmcnt(12)\n\tv_smfmac_f32_32x32x32_bf16 v[0:15], v[104:107], v[56:63], %[idx]\n\t"
          "v_exp_f32 v66, v66\n\tv_exp_f32 v67, v67\n\tv_exp_f32 v68, v68\n\tbuffer_load_dwordx4 v[156:159], %[l16], %[desc], %[sw1] offen offset:3072 sc1\n\t"
          "s_add_u32 %[sw1], %[sw1], 4096\n\tv_exp_f32 v69, v69\n\tv_exp_f32 v70, v70\n\tv_exp_f32 v71, v71\n\t"
          "v_exp_f32 v72, v72\n\tv_exp_f32 v73, v73\n\ts_waitcnt vmcnt(11)\n\tv_smfmac_f32_32x32x32_bf16 v[0:15], v[108:111], v[64:71], %[idx]\n\t"
          "v_exp_f32 v74, v74\n\tv_exp_f32 v75, v75\n\tv_exp_f32 v76, v76\n\ts_waitcnt vmcnt(10)\n\t"
          "v_mfma_f32_32x32x16_bf16 v[56:71], v[112:115], v[48:51], 0\n\tv_exp_f32 v77, v77\n\tv_exp_f32 v78, v78\n\tv_exp_f32 v79, v79\n\t"
          "v_exp_f32 v80, v80\n\tv_exp_f32 v81, v81\n\tv_smfmac_f32_32x32x32_bf16 v[16:31], v[104:107], v[72:79], %[idx]\n\tv_exp_f32 v82, v82\n\t"
          "v_exp_f32 v83, v83\n\ts_mov_b64 exec, %[msk]\n\tbuffer_load_dwordx4 v[104:107], %[vov], %[desc], %[skv] offen offset:3072 sc1\n\ts_mov_b64 exec, -1\n\t"
          "v_exp_f32 v84, v84\n\tv_exp_f32 v85, v85\n\tbuffer_load_dwordx4 v[160:163], %[l16], %[desc], %[sw1] offen offset:0 sc1\n\tv_exp_f32 v86, v86\n\t"
          "v_exp_f32 v87, v87\n\tv_exp_f32 v56, v56\n\tv_exp_f32 v57, v57\n\tv_smfmac_f32_32x32x32_bf16 v[16:31], v[108:111], v[80:87], %[idx]\n\t"
          "v_exp_f32 v58, v58\n\tv_exp_f32 v59, v59\n\ts_mov_b64 exec, %[msk]\n\tbuffer_load_dwordx4 v[108:111], %[vov], %[desc], %[skv] offen offset:3584 sc1\n\t"
          "s_mov_b64 exec, -1\n\ts_add_u32 %[skv], %[skv], 4096\n\tv_exp_f32 v60, v60\n\tv_mfma_f32_32x32x16_bf16 v[72:87], v[112:115], v[52:55], 0\n\t"
          "v_exp_f32 v61, v61\n\tv_exp_f32 v62, v62\n\tbuffer_load_dwordx4 v[112:115], %[l16], %[desc], %[skv] offen offset:0 sc1\n\tv_exp_f32 v63, v63\n\t"
          "v_exp_f32 v64, v64\n\tv_exp_f32 v65, v65\n\ts_waitcnt vmcnt(13)\n\tv_smfmac_f32_32x32x32_bf16 v[0:15], v[116:119], v[56:63], %[idx]\n\t"
          "v_exp_f32 v66, v66\n\tv_exp_f32 v67, v67\n\tv_exp_f32 v68, v68\n\tbuffer_load_dwordx4 v[164:167], %[l16], %[desc], %[sw1] offen offset:1024 sc1\n\t"
          "v_exp_f32 v69, v69\n\tv_exp_f32 v70, v70\n\tv_exp_f32 v71, v71\n\tv_exp_f32 v72, v72\n\t"
          "v_exp_f32 v73, v73\n\ts_waitcnt vmcnt(11)\n\tv_smfmac_f32_32x32x32_bf16 v[0:15], v[120:123], v[64:71], %[idx]\n\tv_exp_f32 v74, v74\n\t"
          "v_exp_f32 v75, v75\n\tv_exp_f32 v76, v76\n\ts_waitcnt vmcnt(10)\n\tv_mfma_f32_32x32x16_bf16 v[56:71], v[88:91], v[48:51], 0\n\t"
          "v_exp_f32 v77, v77\n\tv_exp_f32 v78, v78\n\tv_exp_f32 v79, v79\n\tv_exp_f32 v80, v80\n\t"
          "v_exp_f32 v81, v81\n\tv_smfmac_f32_32x32x32_bf16 v[16:31], v[116:119], v[72:79], %[idx]\n\tv_exp_f32 v82, v82\n\tv_exp_f32 v83, v83\n\t"
          "s_mov_b64 exec, %[msk]\n\tbuffer_load_dwordx4 v[116:119], %[vov], %[desc], %[skv] offen offset:1024 sc1\n\ts_mov_b64 exec, -1\n\tv_exp_f32 v84, v84\n\t"
          "v_exp_f32 v85, v85\n\tv_exp_f32 v86, v86\n\tbuffer_load_dwordx4 %[l16], %[desc], %[swo] offen offset:2048 sc1 lds\n\tv_exp_f32 v87, v87\n\t"
          "v_exp_f32 v56, v56\n\tv_exp_f32 v57, v57\n\tv_smfmac_f32_32x32x32_bf16 v[16:31], v[120:123], v[80:87], %[idx]\n\tv_exp_f32 v58, v58\n\t"
          "v_exp_f32 v59, v59\n\ts_mov_b64 exec, %[msk]\n\tbuffer_load_dwordx4 v[120:123], %[vov], %[desc], %[skv] offen offset:1536 sc1\n\ts_mov_b64 exec, -1\n\t"
          "v_exp_f32 v60, v60\n\tv_mfma_f32_32x32x16_bf16 v[72:87], v[88:91], v[52:55], 0\n\tv_exp_f32 v61, v61\n\tv_exp_f32 v62, v62\n\t"
          "buffer_load_dwordx4 v[88:91], %[l16], %[desc], %[skv] offen offset:2048 sc1\n\tv_exp_f32 v63, v63\n\tv_exp_f32 v64, v64\n\tv_exp_f32 v65, v65\n\t"
          "s_waitcnt vmcnt(12)\n\tv_smfmac_f32_32x32x32_bf16 v[0:15], v[92:95], v[56:63], %[idx]\n\tv_exp_f32 v66, v66\n\tv_exp_f32 v67, v67\n\t"
          "v_exp_f32 v68, v68\n\tbuffer_load_dwordx4 v[168:171], %[l16], %[desc], %[sw1] offen offset:2048 sc1\n\tv_exp_f32 v69, v69\n\tv_exp_f32 v70, v70\n\t"
          "v_exp_f32 v71, v71\n\tv_exp_f32 v72, v72\n\tv_exp_f32 v73, v73\n\ts_waitcnt vmcnt(12)\n\t"
          "v_smfmac_f32_32x32x32_bf16 v[0:15], v[96:99], v[64:71], %[idx]\n\tv_exp_f32 v74, v74\n\tv_exp_f32 v75, v75\n\tv_exp_f32 v76, v76\n\t"
          "s_waitcnt vmcnt(11)\n\tv_mfma_f32_32x32x16_bf16 v[56:71], v[100:103], v[48:51], 0\n\tv_exp_f32 v77, v77\n\tv_exp_f32 v78, v78\n\t"
          "v_exp_f32 v79, v79\n\tv_exp_f32 v80, v80\n\tv_exp_f32 v81, v81\n\tv_smfmac_f32_32x32x32_bf16 v[16:31], v[92:95], v[72:79], %[idx]\n\t"
          "v_exp_f32 v82, v82\n\tv_exp_f32 v83, v83\n\ts_mov_b64 exec, %[msk]\n\tbuffer_load_dwordx4 v[92:95], %[vov], %[desc], %[skv] offen offset:3072 sc1\n\t"
          "s_mov_b64 exec, -1\n\tv_exp_f32 v84, v84\n\tv_exp_f32 v85, v85\n\tbuffer_load_dwordx4 v[172:175], %[l16], %[desc], %[sw1] offen offset:3072 sc1\n\t"
          "s_add_u32 %[sw1], %[sw1], 4096\n\tv_exp_f32 v86, v86\n\tv_exp_f32 v87, v87\n\tv_exp_f32 v56, v56\n\t"
          "v_exp_f32 v57, v57\n\tv_smfmac_f32_32x32x32_bf16 v[16:31], v[96:99], v[80:87], %[idx]\n\tv_exp_f32 v58, v58\n\tv_exp_f32 v59, v59\n\t"
          "s_mov_b64 exec, %[msk]\n\tbuffer_load_dwordx4 v[96:99], %[vov], %[desc], %[skv] offen offset:3584 sc1\n\ts_mov_b64 exec, -1\n\ts_add_u32 %[skv], %[skv], 4096\n\t"
          "v_exp_f32 v60, v60\n\tv_mfma_f32_32x32x16_bf16 v[72:87], v[100:103], v[52:55], 0\n\tv_exp_f32 v61, v61\n\tv_exp_f32 v62, v62\n\t"
          "buffer_load_dwordx4 v[100:103], %[l16], %[desc], %[skv] offen offset:0 sc1\n\tv_exp_f32 v63, v63\n\tv_exp_f32 v64, v64\n\tv_exp_f32 v65, v65\n\t"
          "s_waitcnt vmcnt(13)\n\tv_smfmac_f32_32x32x32_bf16 v[0:15], v[104:107], v[56:63], %[idx]\n\tv_exp_f32 v66, v66\n\tv_exp_f32 v67, v67\n\t"
          "v_exp_f32 v68, v68\n\tv_exp_f32 v69, v69\n\tv_exp_f32 v70, v70\n\tv_exp_f32 v71, v71\n\t"
          "v_exp_f32 v72, v72\n\tv_exp_f32 v73, v73\n\ts_waitcnt vmcnt(11)\n\tv_smfmac_f32_32x32x32_bf16 v[0:15], v[108:111], v[64:71], %[idx]\n\t"
          "v_exp_f32 v74, v74\n\tv_exp_f32 v75, v75\n\tv_exp_f32 v76, v76\n\ts_waitcnt vmcnt(10)\n\t"
          "v_mfma_f32_32x32x16_bf16 v[56:71], v[112:115], v[48:51], 0\n\tv_exp_f32 v77, v77\n\tv_exp_f32 v78, v78\n\tv_exp_f32 v79, v79\n\t"
          "v_exp_f32 v80, v80\n\tv_exp_f32 v81, v81\n\tv_smfmac_f32_32x32x32_bf16 v[16:31], v[104:107], v[72:79], %[idx]\n\tv_exp_f32 v82, v82\n\t"
          "v_exp_f32 v83, v83\n\ts_mov_b64 exec, %[msk]\n\tbuffer_load_dwordx4 v[104:107], %[vov], %[desc], %[skv] offen offset:1024 sc1\n\ts_mov_b64 exec, -1\n\t"
          "v_exp_f32 v84, v84\n\tv_exp_f32 v85, v85\n\tbuffer_load_dwordx4 v[176:179], %[l16], %[desc], %[sw1] offen offset:0 sc1\n\tv_exp_f32 v86, v86\n\t"
          "v_exp_f32 v87, v87\n\tv_exp_f32 v56, v56\n\tv_exp_f32 v57, v57\n\tv_smfmac_f32_32x32x32_bf16 v[16:31], v[108:111], v[80:87], %[idx]\n\t"
          "v_exp_f32 v58, v58\n\tv_exp_f32 v59, v59\n\ts_mov_b64 exec, %[msk]\n\tbuffer_load_dwordx4 v[108:111], %[vov], %[desc], %[skv] offen offset:1536 sc1\n\t"
          "s_mov_b64 exec, -1\n\tv_exp_f32 v60, v60\n\tv_mfma_f32_32x32x16_bf16 v[72:87], v[112:115], v[52:55], 0\n\tv_exp_f32 v61, v61\n\t"
          "v_exp_f32 v62, v62\n\tbuffer_load_dwordx4 v[112:115], %[l16], %[desc], %[skv] offen offset:2048 sc1\n\tv_exp_f32 v63, v63\n\tv_exp_f32 v64, v64\n\t"
          "v_exp_f32 v65, v65\n\ts_waitcnt vmcnt(12)\n\tv_smfmac_f32_32x32x32_bf16 v[0:15], v[116:119], v[56:63], %[idx]\n\tv_exp_f32 v66, v66\n\t"
          "v_exp_f32 v67, v67\n\tv_exp_f32 v68, v68\n\tbuffer_load_dwordx4 v[180:183], %[l16], %[desc], %[sw1] offen offset:1024 sc1\n\tv_exp_f32 v69, v69\n\t"
          "v_exp_f32 v70, v70\n\tv_exp_f32 v71, v71\n\tv_exp_f32 v72, v72\n\tv_exp_f32 v73, v73\n\t"
          "s_waitcnt vmcnt(11)\n\tv_smfmac_f32_32x32x32_bf16 v[0:15], v[120:123], v[64:71], %[idx]\n\tv_exp_f32 v74, v74\n\tv_exp_f32 v75, v75\n\t"
          "v_exp_f32 v76, v76\n\ts_waitcnt vmcnt(10)\n\tv_mfma_f32_32x32x16_bf16 v[56:71], v[88:91], v[48:51], 0\n\tv_exp_f32 v77, v77\n\t"
          "v_exp_f32 v78, v78\n\tv_exp_f32 v79, v79\n\tv_exp_f32 v80, v80\n\tv_exp_f32 v81, v81\n\t"
          "v_smfmac_f32_32x32x32_bf16 v[16:31], v[116:119], v[72:79], %[idx]\n\tv_exp_f32 v82, v82\n\tv_exp_f32 v83, v83\n\ts_mov_b64 exec, %[msk]\n\t"
          "buffer_load_dwordx4 v[116:119], %[vov], %[desc], %[skv] offen offset:3072 sc1\n\ts_mov_b64 exec, -1\n\tv_exp_f32 v84, v84\n\tv_exp_f32 v85, v85\n\t"
          "v_exp_f32 v86, v86\n\tbuffer_load_dwordx4 %[l16], %[desc], %[swo] offen offset:3072 sc1 lds\n\tv_exp_f32 v87, v87\n\tv_exp_f32 v56, v56\n\t"
          "v_exp_f32 v57, v57\n\tv_smfmac_f32_32x32x32_bf16 v[16:31], v[120:123], v[80:87], %[idx]\n\tv_exp_f32 v58, v58\n\tv_exp_f32 v59, v59\n\t"
          "s_mov_b64 exec, %[msk]\n\tbuffer_load_dwordx4 v[120:123], %[vov], %[desc], %[skv] offen offset:3584 sc1\n\ts_mov_b64 exec, -1\n\ts_add_u32 %[skv], %[skv], 4096\n\t"
          "v_exp_f32 v60, v60\n\tv_mfma_f32_32x32x16_bf16 v[72:87], v[88:91], v[52:55], 0\n\tv_exp_f32 v61, v61\n\tv_exp_f32 v62, v62\n\t"
          "buffer_load_dwordx4 v[88:91], %[l16], %[desc], %[skv] offen offset:0 sc1\n\tv_exp_f32 v63, v63\n\tv_exp_f32 v64, v64\n\tv_exp_f32 v65, v65\n\t"
          "s_waitcnt vmcnt(12)\n\tv_smfmac_f32_32x32x32_bf16 v[0:15], v[92:95], v[56:63], %[idx]\n\tv_exp_f32 v66, v66\n\tv_exp_f32 v67, v67\n\t"
          "v_exp_f32 v68, v68\n\tbuffer_load_dwordx4 v[184:187], %[l16], %[desc], %[sw1] offen offset:2048 sc1\n\tv_exp_f32 v69, v69\n\tv_exp_f32 v70, v70\n\t"
          "v_exp_f32 v71, v71\n\tv_exp_f32 v72, v72\n\tv_exp_f32 v73, v73\n\ts_waitcnt vmcnt(11)\n\t"
          "v_smfmac_f32_32x32x32_bf16 v[0:15], v[96:99], v[64:71], %[idx]\n\tv_exp_f32 v74, v74\n\tv_exp_f32 v75, v75\n\tv_exp_f32 v76, v76\n\t"
          "s_waitcnt vmcnt(10)\n\tv_mfma_f32_32x32x16_bf16 v[56:71], v[100:103], v[48:51], 0\n\tv_exp_f32 v77, v77\n\tv_exp_f32 v78, v78\n\t"
          "v_exp_f32 v79, v79\n\tv_exp_f32 v80, v80\n\tv_exp_f32 v81, v81\n\tv_smfmac_f32_32x32x32_bf16 v[16:31], v[92:95], v[72:79], %[idx]\n\t"
          "v_exp_f32 v82, v82\n\tv_exp_f32 v83, v83\n\ts_mov_b64 exec, %[msk]\n\tbuffer_load_dwordx4 v[92:95], %[vov], %[desc], %[skv] offen offset:1024 sc1\n\t"
          "s_mov_b64 exec, -1\n\tv_exp_f32 v84, v84\n\tv_exp_f32 v85, v85\n\tbuffer_load_dwordx4 v[188:191], %[l16], %[desc], %[sw1] offen offset:3072 sc1\n\t"
          "s_add_u32 %[sw1], %[sw1], 4096\n\tv_exp_f32 v86, v86\n\tv_exp_f32 v87, v87\n\tv_exp_f32 v56, v56\n\t"
          "v_exp_f32 v57, v57\n\tv_smfmac_f32_32x32x32_bf16 v[16:31], v[96:99], v[80:87], %[idx]\n\tv_exp_f32 v58, v58\n\tv_exp_f32 v59, v59\n\t"
          "s_mov_b64 exec, %[msk]\n\tbuffer_load_dwordx4 v[96:99], %[vov], %[desc], %[skv] offen offset:1536 sc1\n\ts_mov_b64 exec, -1\n\tv_exp_f32 v60, v60\n\t"
          "v_mfma_f32_32x32x16_bf16 v[72:87], v[100:103], v[52:55], 0\n\tv_exp_f32 v61, v61\n\tv_exp_f32 v62, v62\n\tbuffer_load_dwordx4 v[100:103], %[l16], %[desc], %[skv] offen offset:2048 sc1\n\t"
          "v_exp_f32 v63, v63\n\tv_exp_f32 v64, v64\n\tv_exp_f32 v65, v65\n\ts_waitcnt vmcnt(13)\n\t"
          "v_smfmac_f32_32x32x32_bf16 v[0:15], v[104:107], v[56:63], %[idx]\n\tv_exp_f32 v66, v66\n\tv_exp_f32 v67, v67\n\tv_exp_f32 v68, v68\n\t"
          "v_exp_f32 v69, v69\n\tv_exp_f32 v70, v70\n\tv_exp_f32 v71, v71\n\tv_exp_f32 v72, v72\n\t"
          "v_exp_f32 v73, v73\n\ts_waitcnt vmcnt(11)\n\tv_smfmac_f32_32x32x32_bf16 v[0:15], v[108:111], v[64:71], %[idx]\n\tv_exp_f32 v74, v74\n\t"
          "v_exp_f32 v75, v75\n\tv_exp_f32 v76, v76\n\ts_waitcnt vmcnt(10)\n\tv_mfma_f32_32x32x16_bf16 v[56:71], v[112:115], v[48:51], 0\n\t"
          "v_exp_f32 v77, v77\n\tv_exp_f32 v78, v78\n\tv_exp_f32 v79, v79\n\tv_exp_f32 v80, v80\n\t"
          "v_exp_f32 v81, v81\n\tv_smfmac_f32_32x32x32_bf16 v[16:31], v[104:107], v[72:79], %[idx]\n\tv_exp_f32 v82, v82\n\tv_exp_f32 v83, v83\n\t"
          "s_mov_b64 exec, %[msk]\n\tbuffer_load_dwordx4 v[104:107], %[vov], %[desc], %[skv] offen offset:3072 sc1\n\ts_mov_b64 exec, -1\n\tv_exp_f32 v84, v84\n\t"
          "v_exp_f32 v85, v85\n\tbuffer_load_dwordx4 v[192:195], %[l16], %[desc], %[sw1] offen offset:0 sc1\n\tv_exp_f32 v86, v86\n\tv_exp_f32 v87, v87\n\t"
          "v_exp_f32 v56, v56\n\tv_exp_f32 v57, v57\n\tv_smfmac_f32_32x32x32_bf16 v[16:31], v[108:111], v[80:87], %[idx]\n\tv_exp_f32 v58, v58\n\t"
          "v_exp_f32 v59, v59\n\ts_mov_b64 exec, %[msk]\n\tbuffer_load_dwordx4 v[108:111], %[vov], %[desc], %[skv] offen offset:3584 sc1\n\ts_mov_b64 exec, -1\n\t"
          "s_add_u32 %[skv], %[skv], 4096\n\tv_exp_f32 v60, v60\n\tv_mfma_f32_32x32x16_bf16 v[72:87], v[112:115], v[52:55], 0\n\tv_exp_f32 v61, v61\n\t"
          "v_exp_f32 v62, v62\n\tbuffer_load_dwordx4 v[112:115], %[l16], %[desc], %[skv] offen offset:0 sc1\n\tv_exp_f32 v63, v63\n\tv_exp_f32 v64, v64\n\t"
          "v_exp_f32 v65, v65\n\ts_waitcnt vmcnt(12)\n\tv_smfmac_f32_32x32x32_bf16 v[0:15], v[116:119], v[56:63], %[idx]\n\tv_exp_f32 v66, v66\n\t"
          "v_exp_f32 v67, v67\n\tv_exp_f32 v68, v68\n\tbuffer_load_dwordx4 v[196:199], %[l16], %[desc], %[sw1] offen offset:1024 sc1\n\tv_exp_f32 v69, v69\n\t"
          "v_exp_f32 v70, v70\n\tv_exp_f32 v71, v71\n\tv_exp_f32 v72, v72\n\tv_exp_f32 v73, v73\n\t"
          "s_waitcnt vmcnt(11)\n\tv_smfmac_f32_32x32x32_bf16 v[0:15], v[120:123], v[64:71], %[idx]\n\tv_exp_f32 v74, v74\n\tv_exp_f32 v75, v75\n\t"
          "v_exp_f32 v76, v76\n\ts_waitcnt vmcnt(10)\n\tv_mfma_f32_32x32x16_bf16 v[56:71], v[88:91], v[48:51], 0\n\tv_exp_f32 v77, v77\n\t"
          "v_exp_f32 v78, v78\n\tv_exp_f32 v79, v79\n\tv_exp_f32 v80, v80\n\tv_exp_f32 v81, v81\n\t"
          "v_smfmac_f32_32x32x32_bf16 v[16:31], v[116:119], v[72:79], %[idx]\n\tv_exp_f32 v82, v82\n\tv_exp_f32 v83, v83\n\ts_mov_b64 exec, %[msk]\n\t"
          "buffer_load_dwordx4 v[116:119], %[vov], %[desc], %[skv] offen offset:1024 sc1\n\ts_mov_b64 exec, -1\n\tv_exp_f32 v84, v84\n\tv_exp_f32 v85, v85\n\t"
          "buffer_load_dwordx4 v[200:203], %[l16], %[desc], %[sw1] offen offset:2048 sc1\n\tv_exp_f32 v86, v86\n\ts_add_u32 m0, m0, 4096\n\ts_add_u32 %[swo], %[swo], 4096\n\t"
          "buffer_load_dwordx4 %[l16], %[desc], %[swo] offen offset:0 sc1 lds\n\tv_exp_f32 v87, v87\n\tv_exp_f32 v56, v56\n\tv_exp_f32 v57, v57\n\t"
          "v_smfmac_f32_32x32x32_bf16 v[16:31], v[120:123], v[80:87], %[idx]\n\tv_exp_f32 v58, v58\n\tv_exp_f32 v59, v59\n\ts_mov_b64 exec, %[msk]\n\t"
          "buffer_load_dwordx4 v[120:123], %[vov], %[desc], %[skv] offen offset:1536 sc1\n\ts_mov_b64 exec, -1\n\tv_exp_f32 v60, v60\n\tv_mfma_f32_32x32x16_bf16 v[72:87], v[88:91], v[52:55], 0\n\t"
          "v_exp_f32 v61, v61\n\tv_exp_f32 v62, v62\n\tbuffer_load_dwordx4 v[88:91], %[l16], %[desc], %[skv] offen offset:2048 sc1\n\tv_exp_f32 v63, v63\n\t"
          "v_exp_f32 v64, v64\n\tv_exp_f32 v65, v65\n\ts_waitcnt vmcnt(13)\n\tv_smfmac_f32_32x32x32_bf16 v[0:15], v[92:95], v[56:63], %[idx]\n\t"
          "v_exp_f32 v66, v66\n\tv_exp_f32 v67, v67\n\tv_exp_f32 v68, v68\n\tv_exp_f32 v69, v69\n\t"
          "v_exp_f32 v70, v70\n\tv_exp_f32 v71, v71\n\tv_exp_f32 v72, v72\n\tv_exp_f32 v73, v73\n\t"
          "s_waitcnt vmcnt(11)\n\tv_smfmac_f32_32x32x32_bf16 v[0:15], v[96:99], v[64:71], %[idx]\n\tv_exp_f32 v74, v74\n\tv_exp_f32 v75, v75\n\t"
          "v_exp_f32 v76, v76\n\ts_waitcnt vmcnt(10)\n\tv_mfma_f32_32x32x16_bf16 v[56:71], v[100:103], v[48:51], 0\n\tv_exp_f32 v77, v77\n\t"
          "v_exp_f32 v78, v78\n\tv_exp_f32 v79, v79\n\tv_exp_f32 v80, v80\n\tv_exp_f32 v81, v81\n\t"
          "v_smfmac_f32_32x32x32_bf16 v[16:31], v[92:95], v[72:79], %[idx]\n\tv_exp_f32 v82, v82\n\tv_exp_f32 v83, v83\n\ts_mov_b64 exec, %[msk]\n\t"
          "buffer_load_dwordx4 v[92:95], %[vov], %[desc], %[skv] offen offset:3072 sc1\n\ts_mov_b64 exec, -1\n\tv_exp_f32 v84, v84\n\tv_exp_f32 v85, v85\n\t"
          "buffer_load_dwordx4 v[204:207], %[l16], %[desc], %[sw1] offen offset:3072 sc1\n\ts_add_u32 %[sw1], %[sw1], 4096\n\tv_exp_f32 v86, v86\n\tv_exp_f32 v87, v87\n\t"
          "v_exp_f32 v56, v56\n\tv_exp_f32 v57, v57\n\tv_smfmac_f32_32x32x32_bf16 v[16:31], v[96:99], v[80:87], %[idx]\n\tv_exp_f32 v58, v58\n\t"
          "v_exp_f32 v59, v59\n\ts_mov_b64 exec, %[msk]\n\tbuffer_load_dwordx4 v[96:99], %[vov], %[desc], %[skv] offen offset:3584 sc1\n\ts_mov_b64 exec, -1\n\t"
          "s_add_u32 %[skv], %[skv], 4096\n\tv_exp_f32 v60, v60\n\tv_mfma_f32_32x32x16_bf16 v[72:87], v[100:103], v[52:55], 0\n\tv_exp_f32 v61, v61\n\t"
          "v_exp_f32 v62, v62\n\tbuffer_load_dwordx4 v[100:103], %[l16], %[desc], %[skv] offen offset:0 sc1\n\tv_exp_f32 v63, v63\n\tv_exp_f32 v64, v64\n\t"
          "v_exp_f32 v65, v65\n\ts_waitcnt vmcnt(13)\n\tv_smfmac_f32_32x32x32_bf16 v[0:15], v[104:107], v[56:63], %[idx]\n\tv_exp_f32 v66, v66\n\t"
          "v_exp_f32 v67, v67\n\tv_exp_f32 v68, v68\n\tbuffer_load_dwordx4 v[208:211], %[l16], %[desc], %[sw1] offen offset:0 sc1\n\tv_exp_f32 v69, v69\n\t"
          "v_exp_f32 v70, v70\n\tv_exp_f32 v71, v71\n\tv_exp_f32 v72, v72\n\tv_exp_f32 v73, v73\n\t"
          "s_waitcnt vmcnt(12)\n\tv_smfmac_f32_32x32x32_bf16 v[0:15], v[108:111], v[64:71], %[idx]\n\tv_exp_f32 v74, v74\n\tv_exp_f32 v75, v75\n\t"
          "v_exp_f32 v76, v76\n\ts_waitcnt vmcnt(11)\n\tv_mfma_f32_32x32x16_bf16 v[56:71], v[112:115], v[48:51], 0\n\tv_exp_f32 v77, v77\n\t"
          "v_exp_f32 v78, v78\n\tv_exp_f32 v79, v79\n\tv_exp_f32 v80, v80\n\tv_exp_f32 v81, v81\n\t"
          "v_smfmac_f32_32x32x32_bf16 v[16:31], v[104:107], v[72:79], %[idx]\n\tv_exp_f32 v82, v82\n\tv_exp_f32 v83, v83\n\ts_mov_b64 exec, %[msk]\n\t"
          "buffer_load_dwordx4 v[104:107], %[vov], %[desc], %[skv] offen offset:1024 sc1\n\ts_mov_b64 exec, -1\n\tv_exp_f32 v84, v84\n\tv_exp_f32 v85, v85\n\t"
          "v_exp_f32 v86, v86\n\tv_exp_f32 v87, v87\n\tv_exp_f32 v56, v56\n\tv_exp_f32 v57, v57\n\t"
          "v_smfmac_f32_32x32x32_bf16 v[16:31], v[108:111], v[80:87], %[idx]\n\tv_exp_f32 v58, v58\n\tv_exp_f32 v59, v59\n\ts_mov_b64 exec, %[msk]\n\t"
          "buffer_load_dwordx4 v[108:111], %[vov], %[desc], %[skv] offen offset:1536 sc1\n\ts_mov_b64 exec, -1\n\tv_exp_f32 v60, v60\n\tv_mfma_f32_32x32x16_bf16 v[72:87], v[112:115], v[52:55], 0\n\t"
          "v_exp_f32 v61, v61\n\tv_exp_f32 v62, v62\n\tbuffer_load_dwordx4 v[112:115], %[l16], %[desc], %[skv] offen offset:2048 sc1\n\tv_exp_f32 v63, v63\n\t"
          "v_exp_f32 v64, v64\n\tv_exp_f32 v65, v65\n\ts_waitcnt vmcnt(12)\n\tv_smfmac_f32_32x32x32_bf16 v[0:15], v[116:119], v[56:63], %[idx]\n\t"
          "v_exp_f32 v66, v66\n\tv_exp_f32 v67, v67\n\tv_exp_f32 v68, v68\n\tbuffer_load_dwordx4 v[212:215], %[l16], %[desc], %[sw1] offen offset:1024 sc1\n\t"
          "v_exp_f32 v69, v69\n\tv_exp_f32 v70, v70\n\tv_exp_f32 v71, v71\n\tv_exp_f32 v72, v72\n\t"
          "v_exp_f32 v73, v73\n\ts_waitcnt vmcnt(10)\n\tv_smfmac_f32_32x32x32_bf16 v[0:15], v[120:123], v[64:71], %[idx]\n\tv_exp_f32 v74, v74\n\t"
          "v_exp_f32 v75, v75\n\tv_exp_f32 v76, v76\n\ts_waitcnt vmcnt(9)\n\tv_mfma_f32_32x32x16_bf16 v[56:71], v[88:91], v[48:51], 0\n\t"
          "v_exp_f32 v77, v77\n\tv_exp_f32 v78, v78\n\tv_exp_f32 v79, v79\n\tv_exp_f32 v80, v80\n\t"
          "v_exp_f32 v81, v81\n\tv_smfmac_f32_32x32x32_bf16 v[16:31], v[116:119], v[72:79], %[idx]\n\tv_exp_f32 v82, v82\n\tv_exp_f32 v83, v83\n\t"
          "s_mov_b64 exec, %[msk]\n\tbuffer_load_dwordx4 v[116:119], %[vov], %[desc], %[skv] offen offset:3072 sc1\n\ts_mov_b64 exec, -1\n\tv_exp_f32 v84, v84\n\t"
          "v_exp_f32 v85, v85\n\tbuffer_load_dwordx4 v[216:219], %[l16], %[desc], %[sw1] offen offset:2048 sc1\n\tv_exp_f32 v86, v86\n\tbuffer_load_dwordx4 %[l16], %[desc], %[swo] offen offset:1024 sc1 lds\n\t"
          "v_exp_f32 v87, v87\n\tv_exp_f32 v56, v56\n\tv_exp_f32 v57, v57\n\tv_smfmac_f32_32x32x32_bf16 v[16:31], v[120:123], v[80:87], %[idx]\n\t"
          "v_exp_f32 v58, v58\n\tv_exp_f32 v59, v59\n\ts_mov_b64 exec, %[msk]\n\tbuffer_load_dwordx4 v[120:123], %[vov], %[desc], %[skv] offen offset:3584 sc1\n\t"
          "s_mov_b64 exec, -1\n\ts_add_u32 %[skv], %[skv], 4096\n\tv_exp_f32 v60, v60\n\tv_mfma_f32_32x32x16_bf16 v[72:87], v[88:91], v[52:55], 0\n\t"
          "v_exp_f32 v61, v61\n\tv_exp_f32 v62, v62\n\tbuffer_load_dwordx4 v[88:91], %[l16], %[desc], %[skv] offen offset:0 sc1\n\tv_exp_f32 v63, v63\n\t"
          "v_exp_f32 v64, v64\n\tv_exp_f32 v65, v65\n\ts_waitcnt vmcnt(13)\n\tv_smfmac_f32_32x32x32_bf16 v[0:15], v[92:95], v[56:63], %[idx]\n\t"
          "v_exp_f32 v66, v66\n\tv_exp_f32 v67, v67\n\tv_exp_f32 v68, v68\n\tv_exp_f32 v69, v69\n\t"
          "v_exp_f32 v70, v70\n\tv_exp_f32 v71, v71\n\tv_exp_f32 v72, v72\n\tv_exp_f32 v73, v73\n\t"
          "s_waitcnt vmcnt(11)\n\tv_smfmac_f32_32x32x32_bf16 v[0:15], v[96:99], v[64:71], %[idx]\n\tv_exp_f32 v74, v74\n\tv_exp_f32 v75, v75\n\t"
          "v_exp_f32 v76, v76\n\ts_waitcnt vmcnt(10)\n\tv_mfma_f32_32x32x16_bf16 v[56:71], v[100:103], v[48:51], 0\n\tv_exp_f32 v77, v77\n\t"
          "v_exp_f32 v78, v78\n\tv_exp_f32 v79, v79\n\tv_exp_f32 v80, v80\n\tv_exp_f32 v81, v81\n\t"
          "v_smfmac_f32_32x32x32_bf16 v[16:31], v[92:95], v[72:79], %[idx]\n\tv_exp_f32 v82, v82\n\tv_exp_f32 v83, v83\n\ts_mov_b64 exec, %[msk]\n\t"
          "buffer_load_dwordx4 v[92:95], %[vov], %[desc], %[skv] offen offset:1024 sc1\n\ts_mov_b64 exec, -1\n\tv_exp_f32 v84, v84\n\tv_exp_f32 v85, v85\n\t"
          "buffer_load_dwordx4 v[220:223], %[l16], %[desc], %[sw1] offen offset:3072 sc1\n\ts_add_u32 %[sw1], %[sw1], 4096\n\tv_exp_f32 v86, v86\n\tv_exp_f32 v87, v87\n\t"
          "v_exp_f32 v56, v56\n\ts_setprio 0\n\tv_exp_f32 v57, v57\n\tv_smfmac_f32_32x32x32_bf16 v[16:31], v[96:99], v[80:87], %[idx]\n\t"
          "v_exp_f32 v58, v58\n\tv_exp_f32 v59, v59\n\ts_mov_b64 exec, %[msk]\n\tbuffer_load_dwordx4 v[96:99], %[vov], %[desc], %[skv] offen offset:1536 sc1\n\t"
          "s_mov_b64 exec, -1\n\tv_exp_f32 v60, v60\n\tv_mfma_f32_32x32x16_bf16 v[72:87], v[100:103], v[52:55], 0\n\tv_exp_f32 v61, v61\n\t"
          "v_exp_f32 v62, v62\n\tbuffer_load_dwordx4 v[100:103], %[l16], %[desc], %[skv] offen offset:2048 sc1\n\tv_exp_f32 v63, v63\n\tv_exp_f32 v64, v64\n\t"
          "v_exp_f32 v65, v65\n\ts_waitcnt vmcnt(12)\n\tv_smfmac_f32_32x32x32_bf16 v[0:15], v[104:107], v[56:63], %[idx]\n\tv_exp_f32 v66, v66\n\t"
          "v_exp_f32 v67, v67\n\tv_exp_f32 v68, v68\n\tbuffer_load_dwordx4 v[224:227], %[l16], %[desc], %[sw1] offen offset:0 sc1\n\tv_exp_f32 v69, v69\n\t"
          "v_exp_f32 v70, v70\n\tv_exp_f32 v71, v71\n\tv_exp_f32 v72, v72\n\tv_exp_f32 v73, v73\n\t"
          "s_waitcnt vmcnt(12)\n\tv_smfmac_f32_32x32x32_bf16 v[0:15], v[108:111], v[64:71], %[idx]\n\tv_exp_f32 v74, v74\n\tv_exp_f32 v75, v75\n\t"
          "v_exp_f32 v76, v76\n\ts_waitcnt vmcnt(11)\n\tv_mfma_f32_32x32x16_bf16 v[56:71], v[112:115], v[48:51], 0\n\tv_exp_f32 v77, v77\n\t"
          "v_exp_f32 v78, v78\n\tv_exp_f32 v79, v79\n\tv_exp_f32 v80, v80\n\tv_exp_f32 v81, v81\n\t"
          "v_smfmac_f32_32x32x32_bf16 v[16:31], v[104:107], v[72:79], %[idx]\n\tv_exp_f32 v82, v82\n\tv_exp_f32 v83, v83\n\ts_mov_b64 exec, %[msk]\n\t"
          "buffer_load_dwordx4 v[104:107], %[vov], %[desc], %[skv] offen offset:3072 sc1\n\ts_mov_b64 exec, -1\n\tv_exp_f32 v84, v84\n\tv_exp_f32 v85, v85\n\t"
          "buffer_load_dwordx4 v[228:231], %[l16], %[desc], %[sw1] offen offset:1024 sc1\n\tv_exp_f32 v86, v86\n\tv_exp_f32 v87, v87\n\tv_exp_f32 v56, v56\n\t"
          "v_exp_f32 v57, v57\n\tv_smfmac_f32_32x32x32_bf16 v[16:31], v[108:111], v[80:87], %[idx]\n\tv_exp_f32 v58, v58\n\tv_exp_f32 v59, v59\n\t"
          "s_mov_b64 exec, %[msk]\n\tbuffer_load_dwordx4 v[108:111], %[vov], %[desc], %[skv] offen offset:3584 sc1\n\ts_mov_b64 exec, -1\n\ts_add_u32 %[skv], %[skv], 4096\n\t"
          "v_exp_f32 v60, v60\n\tv_mfma_f32_32x32x16_bf16 v[72:87], v[112:115], v[52:55], 0\n\tv_exp_f32 v61, v61\n\tv_exp_f32 v62, v62\n\t"
          "buffer_load_dwordx4 v[112:115], %[l16], %[desc], %[skv] offen offset:0 sc1\n\tv_exp_f32 v63, v63\n\tv_exp_f32 v64, v64\n\tv_exp_f32 v65, v65\n\t"
          "s_waitcnt vmcnt(13)\n\tv_smfmac_f32_32x32x32_bf16 v[0:15], v[116:119], v[56:63], %[idx]\n\tv_exp_f32 v66, v66\n\tv_exp_f32 v67, v67\n\t"
          "v_exp_f32 v68, v68\n\tv_exp_f32 v69, v69\n\tv_exp_f32 v70, v70\n\tv_exp_f32 v71, v71\n\t"
          "v_exp_f32 v72, v72\n\tv_exp_f32 v73, v73\n\ts_waitcnt vmcnt(10)\n\tv_smfmac_f32_32x32x32_bf16 v[0:15], v[120:123], v[64:71], %[idx]\n\t"
          "v_exp_f32 v74, v74\n\tv_exp_f32 v75, v75\n\tv_exp_f32 v76, v76\n\ts_waitcnt vmcnt(9)\n\t"
          "v_mfma_f32_32x32x16_bf16 v[56:71], v[88:91], v[48:51], 0\n\tv_exp_f32 v77, v77\n\tv_exp_f32 v78, v78\n\tv_exp_f32 v79, v79\n\t"
          "v_exp_f32 v80, v80\n\tbuffer_load_dwordx4 v[44:47], %[l16], %[desc], %[swm] offen offset:0 sc1\n\tv_exp_f32 v81, v81\n\tv_smfmac_f32_32x32x32_bf16 v[16:31], v[116:119], v[72:79], %[idx]\n\t"
          "v_exp_f32 v82, v82\n\tv_exp_f32 v83, v83\n\ts_mov_b64 exec, %[msk]\n\tbuffer_load_dwordx4 v[116:119], %[vov], %[desc], %[skv] offen offset:1024 sc1\n\t"
          "s_mov_b64 exec, -1\n\tv_exp_f32 v84, v84\n\tv_exp_f32 v85, v85\n\tbuffer_load_dwordx4 v[232:235], %[l16], %[desc], %[sw1] offen offset:2048 sc1\n\t"
          "v_exp_f32 v86, v86\n\tbuffer_load_dwordx4 %[l16], %[desc], %[swo] offen offset:2048 sc1 lds\n\tv_exp_f32 v87, v87\n\tv_exp_f32 v56, v56\n\t"
          "v_exp_f32 v57, v57\n\tv_smfmac_f32_32x32x32_bf16 v[16:31], v[120:123], v[80:87], %[idx]\n\tv_exp_f32 v58, v58\n\tv_exp_f32 v59, v59\n\t"
          "s_mov_b64 exec, %[msk]\n\tbuffer_load_dwordx4 v[120:123], %[vov], %[desc], %[skv] offen offset:1536 sc1\n\ts_mov_b64 exec, -1\n\tv_exp_f32 v60, v60\n\t"
          "v_mfma_f32_32x32x16_bf16 v[72:87], v[88:91], v[52:55], 0\n\tv_exp_f32 v61, v61\n\tv_exp_f32 v62, v62\n\tbuffer_load_dwordx4 v[88:91], %[l16], %[desc], %[skv] offen offset:2048 sc1\n\t"
          "v_exp_f32 v63, v63\n\tv_exp_f32 v64, v64\n\tv_exp_f32 v65, v65\n\ts_waitcnt vmcnt(14)\n\t"
          "v_smfmac_f32_32x32x32_bf16 v[0:15], v[92:95], v[56:63], %[idx]\n\tv_exp_f32 v66, v66\n\tv_exp_f32 v67, v67\n\tv_exp_f32 v68, v68\n\t"
          "buffer_load_dwordx4 v[236:239], %[l16], %[desc], %[sw1] offen offset:3072 sc1\n\ts_add_u32 %[sw1], %[sw1], 4096\n\tv_exp_f32 v69, v69\n\tv_exp_f32 v70, v70\n\t"
          "v_exp_f32 v71, v71\n\tv_exp_f32 v72, v72\n\tv_exp_f32 v73, v73\n\ts_waitcnt vmcnt(13)\n\t"
          "v_smfmac_f32_32x32x32_bf16 v[0:15], v[96:99], v[64:71], %[idx]\n\tv_exp_f32 v74, v74\n\tv_exp_f32 v75, v75\n\tv_exp_f32 v76, v76\n\t"
          "s_waitcnt vmcnt(12)\n\tv_mfma_f32_32x32x16_bf16 v[56:71], v[100:103], v[48:51], 0\n\tv_exp_f32 v77, v77\n\tv_exp_f32 v78, v78\n\t"
          "v_exp_f32 v79, v79\n\tv_exp_f32 v80, v80\n\tbuffer_load_dwordx4 v[44:47], %[l16], %[desc], %[swm] offen offset:1024 sc1\n\tv_exp_f32 v81, v81\n\t"
          "v_smfmac_f32_32x32x32_bf16 v[16:31], v[92:95], v[72:79], %[idx]\n\tv_exp_f32 v82, v82\n\tv_exp_f32 v83, v83\n\ts_mov_b64 exec, %[msk]\n\t"
          "buffer_load_dwordx4 v[92:95], %[vov], %[desc], %[skv] offen offset:3072 sc1\n\ts_mov_b64 exec, -1\n\tv_exp_f32 v84, v84\n\tv_exp_f32 v85, v85\n\t"
          "v_exp_f32 v86, v86\n\tv_exp_f32 v87, v87\n\tv_exp_f32 v56, v56\n\tv_exp_f32 v57, v57\n\t"
          "v_smfmac_f32_32x32x32_bf16 v[16:31], v[96:99], v[80:87], %[idx]\n\tv_exp_f32 v58, v58\n\tv_exp_f32 v59, v59\n\ts_mov_b64 exec, %[msk]\n\t"
          "buffer_load_dwordx4 v[96:99], %[vov], %[desc], %[skv] offen offset:3584 sc1\n\ts_mov_b64 exec, -1\n\ts_add_u32 %[skv], %[skv], 4096\n\tv_exp_f32 v60, v60\n\t"
          "v_mfma_f32_32x32x16_bf16 v[72:87], v[100:103], v[52:55], 0\n\tv_exp_f32 v61, v61\n\tv_exp_f32 v62, v62\n\tbuffer_load_dwordx4 v[100:103], %[l16], %[desc], %[skv] offen offset:0 sc1\n\t"
          "v_exp_f32 v63, v63\n\tv_exp_f32 v64, v64\n\tv_exp_f32 v65, v65\n\ts_waitcnt vmcnt(14)\n\t"
          "v_smfmac_f32_32x32x32_bf16 v[0:15], v[104:107], v[56:63], %[idx]\n\tv_exp_f32 v66, v66\n\tv_exp_f32 v67, v67\n\tv_exp_f32 v68, v68\n\t"
          "buffer_load_dwordx4 v[240:243], %[l16], %[desc], %[sw1] offen offset:0 sc1\n\tv_exp_f32 v69, v69\n\tv_exp_f32 v70, v70\n\tv_exp_f32 v71, v71\n\t"
          "v_exp_f32 v72, v72\n\tv_exp_f32 v73, v73\n\ts_waitcnt vmcnt(13)\n\tv_smfmac_f32_32x32x32_bf16 v[0:15], v[108:111], v[64:71], %[idx]\n\t"
          "v_exp_f32 v74, v74\n\tv_exp_f32 v75, v75\n\tv_exp_f32 v76, v76\n\ts_waitcnt vmcnt(12)\n\t"
          "v_mfma_f32_32x32x16_bf16 v[56:71], v[112:115], v[48:51], 0\n\tv_exp_f32 v77, v77\n\tv_exp_f32 v78, v78\n\tv_exp_f32 v79, v79\n\t"
          "v_exp_f32 v80, v80\n\tbuffer_load_dwordx4 v[44:47], %[l16], %[desc], %[swm] offen offset:2048 sc1\n\tv_exp_f32 v81, v81\n\tv_smfmac_f32_32x32x32_bf16 v[16:31], v[104:107], v[72:79], %[idx]\n\t"
          "v_exp_f32 v82, v82\n\tv_exp_f32 v83, v83\n\ts_mov_b64 exec, %[msk]\n\tbuffer_load_dwordx4 v[104:107], %[vov], %[desc], %[skv] offen offset:1024 sc1\n\t"
          "s_mov_b64 exec, -1\n\tv_exp_f32 v84, v84\n\tv_exp_f32 v85, v85\n\tbuffer_load_dwordx4 v[244:247], %[l16], %[desc], %[sw1] offen offset:1024 sc1\n\t"
          "v_exp_f32 v86, v86\n\tv_exp_f32 v87, v87\n\tv_exp_f32 v56, v56\n\tv_exp_f32 v57, v57\n\t"
          "v_smfmac_f32_32x32x32_bf16 v[16:31], v[108:111], v[80:87], %[idx]\n\tv_exp_f32 v58, v58\n\tv_exp_f32 v59, v59\n\ts_mov_b64 exec, %[msk]\n\t"
          "buffer_load_dwordx4 v[108:111], %[vov], %[desc], %[skv] offen offset:1536 sc1\n\ts_mov_b64 exec, -1\n\tv_exp_f32 v60, v60\n\tv_mfma_f32_32x32x16_bf16 v[72:87], v[112:115], v[52:55], 0\n\t"
          "v_exp_f32 v61, v61\n\tv_exp_f32 v62, v62\n\tbuffer_load_dwordx4 v[112:115], %[l16], %[desc], %[skv] offen offset:2048 sc1\n\tv_exp_f32 v63, v63\n\t"
          "v_exp_f32 v64, v64\n\tv_exp_f32 v65, v65\n\ts_waitcnt vmcnt(15)\n\tv_smfmac_f32_32x32x32_bf16 v[0:15], v[116:119], v[56:63], %[idx]\n\t"
          "v_exp_f32 v66, v66\n\tv_exp_f32 v67, v67\n\tv_exp_f32 v68, v68\n\tv_exp_f32 v69, v69\n\t"
          "v_exp_f32 v70, v70\n\tv_exp_f32 v71, v71\n\tv_exp_f32 v72, v72\n\tv_exp_f32 v73, v73\n\t"
          "s_waitcnt vmcnt(12)\n\tv_smfmac_f32_32x32x32_bf16 v[0:15], v[120:123], v[64:71], %[idx]\n\tv_exp_f32 v74, v74\n\tv_exp_f32 v75, v75\n\t"
          "v_exp_f32 v76, v76\n\ts_waitcnt vmcnt(11)\n\tv_mfma_f32_32x32x16_bf16 v[56:71], v[88:91], v[48:51], 0\n\tv_exp_f32 v77, v77\n\t"
          "v_exp_f32 v78, v78\n\tv_exp_f32 v79, v79\n\tv_exp_f32 v80, v80\n\tbuffer_load_dwordx4 v[44:47], %[l16], %[desc], %[swm] offen offset:3072 sc1\n\t"
          "s_add_u32 %[swm], %[swm], 4096\n\tv_exp_f32 v81, v81\n\tv_smfmac_f32_32x32x32_bf16 v[16:31], v[116:119], v[72:79], %[idx]\n\tv_exp_f32 v82, v82\n\t"
          "v_exp_f32 v83, v83\n\ts_mov_b64 exec, %[msk]\n\tbuffer_load_dwordx4 v[116:119], %[vov], %[desc], %[skv] offen offset:3072 sc1\n\ts_mov_b64 exec, -1\n\t"
          "v_exp_f32 v84, v84\n\tv_exp_f32 v85, v85\n\tbuffer_load_dwordx4 v[248:251], %[l16], %[desc], %[sw1] offen offset:2048 sc1\n\tv_exp_f32 v86, v86\n\t"
          "buffer_load_dwordx4 %[l16], %[desc], %[swo] offen offset:3072 sc1 lds\n\tv_exp_f32 v87, v87\n\tv_exp_f32 v56, v56\n\tv_exp_f32 v57, v57\n\t"
          "v_smfmac_f32_32x32x32_bf16 v[16:31], v[120:123], v[80:87], %[idx]\n\tv_exp_f32 v58, v58\n\tv_exp_f32 v59, v59\n\ts_mov_b64 exec, %[msk]\n\t"
          "buffer_load_dwordx4 v[120:123], %[vov], %[desc], %[skv] offen offset:3584 sc1\n\ts_mov_b64 exec, -1\n\ts_add_u32 %[skv], %[skv], 4096\n\tv_exp_f32 v60, v60\n\t"
          "v_mfma_f32_32x32x16_bf16 v[72:87], v[88:91], v[52:55], 0\n\tv_exp_f32 v61, v61\n\tv_exp_f32 v62, v62\n\tbuffer_load_dwordx4 v[88:91], %[l16], %[desc], %[skv] offen offset:0 sc1\n\t"
          "v_exp_f32 v63, v63\n\tv_exp_f32 v64, v64\n\tv_exp_f32 v65, v65\n\ts_waitcnt vmcnt(14)\n\t"
          "v_smfmac_f32_32x32x32_bf16 v[0:15], v[92:95], v[56:63], %[idx]\n\tv_exp_f32 v66, v66\n\tv_exp_f32 v67, v67\n\tv_exp_f32 v68, v68\n\t"
          "buffer_load_dwordx4 v[252:255], %[l16], %[desc], %[sw1] offen offset:3072 sc1\n\tv_exp_f32 v69, v69\n\tv_exp_f32 v70, v70\n\tv_exp_f32 v71, v71\n\t"
          "v_exp_f32 v72, v72\n\tv_exp_f32 v73, v73\n\ts_waitcnt vmcnt(14)\n\tv_smfmac_f32_32x32x32_bf16 v[0:15], v[96:99], v[64:71], %[idx]\n\t"
          "v_exp_f32 v74, v74\n\tv_exp_f32 v75, v75\n\tv_exp_f32 v76, v76\n\ts_waitcnt vmcnt(13)\n\t"
          "v_mfma_f32_32x32x16_bf16 v[56:71], v[100:103], v[48:51], 0\n\tv_exp_f32 v77, v77\n\tv_exp_f32 v78, v78\n\tv_exp_f32 v79, v79\n\t"
          "v_exp_f32 v80, v80\n\tbuffer_load_dwordx4 v[44:47], %[l16], %[desc], %[swm] offen offset:0 sc1\n\tv_exp_f32 v81, v81\n\tv_smfmac_f32_32x32x32_bf16 v[16:31], v[92:95], v[72:79], %[idx]\n\t"
          "v_exp_f32 v82, v82\n\tv_exp_f32 v83, v83\n\ts_mov_b64 exec, %[msk]\n\tbuffer_load_dwordx4 v[92:95], %[vov], %[desc], %[skv] offen offset:1024 sc1\n\t"
          "s_mov_b64 exec, -1\n\tv_exp_f32 v84, v84\n\tv_exp_f32 v85, v85\n\tv_exp_f32 v86, v86\n\t"
          "v_exp_f32 v87, v87\n\tv_exp_f32 v56, v56\n\tv_exp_f32 v57, v57\n\tv_smfmac_f32_32x32x32_bf16 v[16:31], v[96:99], v[80:87], %[idx]\n\t"
          "v_exp_f32 v58, v58\n\tv_exp_f32 v59, v59\n\ts_mov_b64 exec, %[msk]\n\tbuffer_load_dwordx4 v[96:99], %[vov], %[desc], %[skv] offen offset:1536 sc1\n\t"
          "s_mov_b64 exec, -1\n\tv_exp_f32 v60, v60\n\tv_mfma_f32_32x32x16_bf16 v[72:87], v[100:103], v[52:55], 0\n\tv_exp_f32 v61, v61\n\t"
          "v_exp_f32 v62, v62\n\tbuffer_load_dwordx4 v[100:103], %[l16], %[desc], %[skv] offen offset:2048 sc1\n\tv_exp_f32 v63, v63\n\tv_exp_f32 v64, v64\n\t"
          "v_exp_f32 v65, v65\n\ts_waitcnt vmcnt(14)\n\tv_smfmac_f32_32x32x32_bf16 v[0:15], v[104:107], v[56:63], %[idx]\n\tv_exp_f32 v66, v66\n\t"
          "v_exp_f32 v67, v67\n\tv_exp_f32 v68, v68\n\tv_exp_f32 v69, v69\n\tv_exp_f32 v70, v70\n\t"
          "v_exp_f32 v71, v71\n\tv_exp_f32 v72, v72\n\tv_exp_f32 v73, v73\n\ts_waitcnt vmcnt(12)\n\t"
          "v_smfmac_f32_32x32x32_bf16 v[0:15], v[108:111], v[64:71], %[idx]\n\tv_exp_f32 v74, v74\n\tv_exp_f32 v75, v75\n\tv_exp_f32 v76, v76\n\t"
          "s_waitcnt vmcnt(11)\n\tv_mfma_f32_32x32x16_bf16 v[56:71], v[112:115], v[48:51], 0\n\tv_exp_f32 v77, v77\n\tv_exp_f32 v78, v78\n\t"
          "v_exp_f32 v79, v79\n\tv_exp_f32 v80, v80\n\tbuffer_load_dwordx4 v[44:47], %[l16], %[desc], %[swm] offen offset:1024 sc1\n\tv_exp_f32 v81, v81\n\t"
          "v_smfmac_f32_32x32x32_bf16 v[16:31], v[104:107], v[72:79], %[idx]\n\tv_exp_f32 v82, v82\n\tv_exp_f32 v83, v83\n\ts_mov_b64 exec, %[msk]\n\t"
          "buffer_load_dwordx4 v[104:107], %[vov], %[desc], %[skv] offen offset:3072 sc1\n\ts_mov_b64 exec, -1\n\tv_exp_f32 v84, v84\n\tv_exp_f32 v85, v85\n\t"
          "v_exp_f32 v86, v86\n\tv_exp_f32 v87, v87\n\tv_exp_f32 v56, v56\n\tv_exp_f32 v57, v57\n\t"
          "v_smfmac_f32_32x32x32_bf16 v[16:31], v[108:111], v[80:87], %[idx]\n\tv_exp_f32 v58, v58\n\tv_exp_f32 v59, v59\n\ts_mov_b64 exec, %[msk]\n\t"
          "buffer_load_dwordx4 v[108:111], %[vov], %[desc], %[skv] offen offset:3584 sc1\n\ts_mov_b64 exec, -1\n\ts_add_u32 %[skv], %[skv], 4096\n\tv_exp_f32 v60, v60\n\t"
          "v_mfma_f32_32x32x16_bf16 v[72:87], v[112:115], v[52:55], 0\n\tv_exp_f32 v61, v61\n\tv_exp_f32 v62, v62\n\tv_exp_f32 v63, v63\n\t"
          "v_exp_f32 v64, v64\n\tv_exp_f32 v65, v65\n\ts_waitcnt vmcnt(12)\n\tv_smfmac_f32_32x32x32_bf16 v[0:15], v[116:119], v[56:63], %[idx]\n\t"
          "v_exp_f32 v66, v66\n\tv_exp_f32 v67, v67\n\tv_exp_f32 v68, v68\n\tv_exp_f32 v69, v69\n\t"
          "v_exp_f32 v70, v70\n\tv_exp_f32 v71, v71\n\tv_exp_f32 v72, v72\n\tv_exp_f32 v73, v73\n\t"
          "s_waitcnt vmcnt(9)\n\tv_smfmac_f32_32x32x32_bf16 v[0:15], v[120:123], v[64:71], %[idx]\n\tv_exp_f32 v74, v74\n\tv_exp_f32 v75, v75\n\t"
          "v_exp_f32 v76, v76\n\ts_waitcnt vmcnt(8)\n\tv_mfma_f32_32x32x16_bf16 v[56:71], v[88:91], v[48:51], 0\n\tv_exp_f32 v77, v77\n\t"
          "v_exp_f32 v78, v78\n\tv_exp_f32 v79, v79\n\tv_exp_f32 v80, v80\n\tbuffer_load_dwordx4 v[44:47], %[l16], %[desc], %[swm] offen offset:2048 sc1\n\t"
          "v_exp_f32 v81, v81\n\tv_smfmac_f32_32x32x32_bf16 v[16:31], v[116:119], v[72:79], %[idx]\n\tv_exp_f32 v82, v82\n\tv_exp_f32 v83, v83\n\t"
          "v_exp_f32 v84, v84\n\tv_exp_f32 v85, v85\n\tv_exp_f32 v86, v86\n\tv_exp_f32 v87, v87\n\t"
          "v_exp_f32 v56, v56\n\tv_exp_f32 v57, v57\n\tv_smfmac_f32_32x32x32_bf16 v[16:31], v[120:123], v[80:87], %[idx]\n\tv_exp_f32 v58, v58\n\t"
          "v_exp_f32 v59, v59\n\tv_exp_f32 v60, v60\n\tv_mfma_f32_32x32x16_bf16 v[72:87], v[88:91], v[52:55], 0\n\tv_exp_f32 v61, v61\n\t"
          "v_exp_f32 v62, v62\n\tv_exp_f32 v63, v63\n\tv_exp_f32 v64, v64\n\tv_exp_f32 v65, v65\n\t"
          "s_waitcnt vmcnt(6)\n\tv_smfmac_f32_32x32x32_bf16 v[0:15], v[92:95], v[56:63], %[idx]\n\tv_exp_f32 v66, v66\n\tv_exp_f32 v67, v67\n\t"
          "v_exp_f32 v68, v68\n\tv_exp_f32 v69, v69\n\tv_exp_f32 v70, v70\n\tv_exp_f32 v71, v71\n\t"
          "v_exp_f32 v72, v72\n\tv_exp_f32 v73, v73\n\ts_waitcnt vmcnt(5)\n\tv_smfmac_f32_32x32x32_bf16 v[0:15], v[96:99], v[64:71], %[idx]\n\t"
          "v_exp_f32 v74, v74\n\tv_exp_f32 v75, v75\n\tv_exp_f32 v76, v76\n\ts_waitcnt vmcnt(4)\n\t"
          "v_mfma_f32_32x32x16_bf16 v[56:71], v[100:103], v[48:51], 0\n\tv_exp_f32 v77, v77\n\tv_exp_f32 v78, v78\n\tv_exp_f32 v79, v79\n\t"
          "v_exp_f32 v80, v80\n\tv_exp_f32 v81, v81\n\tv_smfmac_f32_32x32x32_bf16 v[16:31], v[92:95], v[72:79], %[idx]\n\tv_exp_f32 v82, v82\n\t"
          "v_exp_f32 v83, v83\n\tv_exp_f32 v84, v84\n\tv_exp_f32 v85, v85\n\tv_exp_f32 v86, v86\n\t"
          "v_exp_f32 v87, v87\n\tv_exp_f32 v56, v56\n\tv_exp_f32 v57, v57\n\tv_smfmac_f32_32x32x32_bf16 v[16:31], v[96:99], v[80:87], %[idx]\n\t"
          "v_exp_f32 v58, v58\n\tv_exp_f32 v59, v59\n\tv_exp_f32 v60, v60\n\tv_mfma_f32_32x32x16_bf16 v[72:87], v[100:103], v[52:55], 0\n\t"
          "v_exp_f32 v61, v61\n\tv_exp_f32 v62, v62\n\tv_exp_f32 v63, v63\n\tv_exp_f32 v64, v64\n\t"
          "v_exp_f32 v65, v65\n\ts_waitcnt vmcnt(2)\n\tv_smfmac_f32_32x32x32_bf16 v[0:15], v[104:107], v[56:63], %[idx]\n\tv_exp_f32 v66, v66\n\t"
          "v_exp_f32 v67, v67\n\tv_exp_f32 v68, v68\n\tv_exp_f32 v69, v69\n\tv_exp_f32 v70, v70\n\t"
          "v_exp_f32 v71, v71\n\tv_exp_f32 v72, v72\n\tv_exp_f32 v73, v73\n\ts_waitcnt vmcnt(1)\n\t"
          "v_smfmac_f32_32x32x32_bf16 v[0:15], v[108:111], v[64:71], %[idx]\n\tv_exp_f32 v74, v74\n\tv_exp_f32 v75, v75\n\tv_exp_f32 v76, v76\n\t"
          "v_exp_f32 v77, v77\n\tv_exp_f32 v78, v78\n\tv_exp_f32 v79, v79\n\tv_exp_f32 v80, v80\n\t"
          "v_exp_f32 v81, v81\n\tv_smfmac_f32_32x32x32_bf16 v[16:31], v[104:107], v[72:79], %[idx]\n\tv_exp_f32 v82, v82\n\tv_exp_f32 v83, v83\n\t"
          "v_exp_f32 v84, v84\n\tv_exp_f32 v85, v85\n\tv_exp_f32 v86, v86\n\tv_exp_f32 v87, v87\n\t"
          "s_nop 1\n\tv_smfmac_f32_32x32x32_bf16 v[16:31], v[108:111], v[80:87], %[idx]\n\tv_add_u32 v56, %[mx], %[l16]\n\tv_xor_b32 v57, 0x4000, v56\n\t"
          "v_mov_b32 v58, %[mfl]\n\tv_mov_b32 v59, 1\n\tv_mov_b64 v[60:61], %[tag]\n\tv_mov_b64 v[62:63], %[tag]\n\t"
          "v_add_f32 v64, |v0|, |v1|\n\tv_add_f32 v65, |v2|, |v3|\n\tv_add_f32 v64, v64, |v4|\n\tv_add_f32 v65, v65, |v5|\n\t"
          "v_add_f32 v64, v64, |v6|\n\tv_add_f32 v65, v65, |v7|\n\ts_nop 1\n\ts_cmp_ge_u32 %[w], 4\n\t"
          "s_cbranch_scc1 AH%=\n\tds_write_b128 v56, v[16:19]\n\tds_write_b128 v56, v[20:23] offset:1024\n\tds_write_b32 v56, v24 offset:2048\n\t"
          "v_add_f32 v66, |v16|, |v17|\n\tv_add_f32 v67, |v18|, |v19|\n\tv_add_f32 v66, v66, |v20|\n\tv_add_f32 v67, v67, |v21|\n\t"
          "v_add_f32 v66, v66, |v22|\n\tv_add_f32 v67, v67, |v23|\n\tv_add_f32 v64, v64, v65\n\tv_add_f32 v66, v66, v67\n\t"
          "v_add_f32 v65, v8, v24\n\tv_add_f32 v64, v64, v66\n\tv_min_f32 v66, v8, v24\n\tv_fmac_f32 v65, 0x21800000, v64\n\t"
          "v_cmp_nle_f32 vcc, 0x2b800000, v66\n\ts_mov_b64 s[94:95], vcc\n\tv_cmp_nge_f32 vcc, 0x5d000000, v65\n\ts_or_b64 s[94:95], s[94:95], vcc\n\t"
          "s_cbranch_scc0 AC%=\n\tds_write_b32 v58, v59\n\tAC%=:\n\ts_waitcnt lgkmcnt(0)\n\t"
          "s_barrier\n\tds_read_b32 v74, v57 offset:2048\n\tds_read_b128 v[66:69], v57\n\tds_read_b128 v[70:73], v57 offset:1024\n\t"
          "ds_read_b32 v59, v58\n\ts_waitcnt lgkmcnt(3)\n\tv_add_f32 v74, v74, v8\n\tv_rcp_f32 v74, v74\n\t"
          "s_waitcnt lgkmcnt(1)\n\tv_pk_add_f32 v[66:67], v[66:67], v[0:1]\n\tv_pk_add_f32 v[68:69], v[68:69], v[2:3]\n\tv_pk_add_f32 v[70:71], v[70:71], v[4:5]\n\t"
          "v_pk_add_f32 v[72:73], v[72:73], v[6:7]\n\ts_branch AJ%=\n\tAH%=:\n\tds_write_b128 v56, v[0:3]\n\t"
          "ds_write_b128 v56, v[4:7] offset:1024\n\tds_write_b32 v56, v8 offset:2048\n\tv_add_f32 v66, |v16|, |v17|\n\tv_add_f32 v67, |v18|, |v19|\n\t"
          "v_add_f32 v66, v66, |v20|\n\tv_add_f32 v67, v67, |v21|\n\tv_add_f32 v66, v66, |v22|\n\tv_add_f32 v67, v67, |v23|\n\t"
          "v_add_f32 v64, v64, v65\n\tv_add_f32 v66, v66, v67\n\tv_add_f32 v65, v8, v24\n\tv_add_f32 v64, v64, v66\n\t"
          "v_min_f32 v66, v8, v24\n\tv_fmac_f32 v65, 0x21800000, v64\n\tv_cmp_nle_f32 vcc, 0x2b800000, v66\n\ts_mov_b64 s[94:95], vcc\n\t"
          "v_cmp_nge_f32 vcc, 0x5d000000, v65\n\ts_or_b64 s[94:95], s[94:95], vcc\n\ts_cbranch_scc0 AD%=\n\tds_write_b32 v58, v59\n\t"
          "AD%=:\n\ts_waitcnt lgkmcnt(0)\n\ts_barrier\n\tds_read_b32 v74, v57 offset:2048\n\t"
          "ds_read_b128 v[66:69], v57\n\tds_read_b128 v[70:73], v57 offset:1024\n\tds_read_b32 v59, v58\n\ts_waitcnt lgkmcnt(3)\n\t"
          "v_add_f32 v74, v74, v24\n\tv_rcp_f32 v74, v74\n\ts_waitcnt lgkmcnt(1)\n\tv_pk_add_f32 v[66:67], v[66:67], v[16:17]\n\t"
          "v_pk_add_f32 v[68:69], v[68:69], v[18:19]\n\tv_pk_add_f32 v[70:71], v[70:71], v[20:21]\n\tv_pk_add_f32 v[72:73], v[72:73], v[22:23]\n\tAJ%=:\n\t"
          "v_pk_mul_f32 v[66:67], v[74:75], v[66:67] op_sel_hi:[0,1]\n\tv_pk_mul_f32 v[68:69], v[74:75], v[68:69] op_sel_hi:[0,1]\n\tv_pk_mul_f32 v[70:71], v[74:75], v[70:71] op_sel_hi:[0,1]\n\tv_pk_mul_f32 v[72:73], v[74:75], v[72:73] op_sel_hi:[0,1]\n\t"
          "v_cvt_pk_bf16_f32 v66, v66, v67\n\tv_cvt_pk_bf16_f32 v67, v68, v69\n\tv_cvt_pk_bf16_f32 v68, v70, v71\n\tv_cvt_pk_bf16_f32 v69, v72, v73\n\t"
          "buffer_store_dwordx4 v[66:69], %[l16], %[desc], %[so] offen sc1\n\ts_waitcnt lgkmcnt(0)\n\tv_readfirstlane_b32 %[bad], v59\n\ts_nop 3\n\t"
          "s_cmp_lg_u32 %[bad], 0\n\ts_cbranch_scc1 AX%=\n\ts_waitcnt vmcnt(0) lgkmcnt(0)\n\ts_barrier\n\t"
          "s_cmp_lg_u32 %[w], 0\n\ts_cbranch_scc1 AY%=\n\ts_mov_b64 exec, 0xff\n\tbuffer_store_dwordx4 v[60:63], %[l16], %[desc], %[sf] offen sc1\n\t"
          "s_mov_b64 exec, -1\n\ts_nop 1\n\tAY%=:\n\ts_nop 7\n\t"
          "AX%=:\n\ts_waitcnt vmcnt(0)\n\t"
            : [skv] "+s"(skv), [sw1] "+s"(sw1), [swo] "+s"(swo), [swm] "+s"(swm), [bud] "+s"(bud), [bad] "=&s"(bad)
            : [l16] "v"(l16), [vov] "v"((h * 16 + (lane & 15)) * 16), [vin] "v"((tok == 16 || tok == 20) ? 0x3F803F80u : 0u), [idx] "v"(0xDDDD),
              [vf] "v"(flag_off(SL_QKV + sl, xc * 32 + tok)),
              [vpf] "v"(flag_off(SL_QKV, ((pf & 1) * 4 + ((pf >> 3) & 3)) * 32 + ((pf & 7) >> 1) * 8 + (pf >> 5))),
              [desc] "s"(desc), [sq] "s"(LB + LO_Q + (head * 64 + 2 * bi) * 1024), [msk] "s"(0x0000FFFF0000FFFFull), [w] "s"(w), [tag] "s"(tag),
              [mwo] "s"(US(lds0 + L_WO + w * 8192)), [mx] "s"(US(lds0 + w * 4096)), [mfl] "s"(US(lds0 + L_FLAG)), [so] "s"(oso), [sf] "s"(osf)
            : "v0", "v1", "v2", "v3", "v4", "v5", "v6", "v7", "v8", "v9", "v10", "v11", "v12", "v13", "v14", "v15", "v16", "v17", "v18", "v19", "v20", "v21", "v22", "v23", "v24", "v25", "v26", "v27", "v28", "v29", "v30", "v31", "v44", "v45", "v46", "v47", "v48", "v49", "v50", "v51", "v52", "v53", "v54", "v55", "v56", "v57", "v58", "v59", "v60", "v61", "v62", "v63", "v64", "v65", "v66", "v67", "v68", "v69", "v70", "v71", "v72", "v73", "v74", "v75", "v76", "v77", "v78", "v79", "v80", "v81", "v82", "v83", "v84", "v85", "v86", "v87", "v88", "v89", "v90", "v91", "v92", "v93", "v94", "v95", "v96", "v97", "v98", "v99", "v100", "v101", "v102", "v103", "v104", "v105", "v106", "v107", "v108", "v109", "v110", "v111", "v112", "v113", "v114", "v115", "v116", "v117", "v118", "v119", "v120", "v121", "v122", "v123", "v124", "v125", "v126", "v127", "s94", "s95", "scc", "vcc", "memory");
      }
      if (US(bad) != 0u) {
        const u32 voV = (h * 16 + (lane & 15)) * 16;
        const u32 vinit = (tok == 16 || tok == 20) ? 0x3F803F80u : 0u;
        const v4u q = ld16(rs, l16, LB + LO_Q + (head * 64 + 2 * bi + half) * 1024);
        f32x16 o = {0};
        float m = -__builtin_inff();
#pragma unroll 1
        for (int t = 0; t < 64; ++t) {
          const u32 so = LB + LO_KV + (head * 64 + t) * 2048;
          const v4u kf = ld16(rs, l16, so);
          v4u v1 = ld16(rs, voV + 1024, so), v2 = ld16(rs, voV + 1536, so);
          if (tok >= 16) { v1 = v4u{vinit, vinit, vinit, vinit}; v2 = v1; }
          f32x16 s = {0};
          s = __builtin_amdgcn_mfma_f32_32x32x16_bf16(__builtin_bit_cast(bf16x8, kf), __builtin_bit_cast(bf16x8, q), s, 0, 0, 0);
          float mx = s[0];
#pragma unroll
          for (int e = 1; e < 16; ++e) mx = fmaxf(mx, s[e]);
          mx = max_halves(mx);
          const float mn = fmaxf(m, mx);
          const float al = __builtin_amdgcn_exp2f(m - mn);
          m = mn;
#pragma unroll
          for (int e = 0; e < 16; ++e) { s[e] = __builtin_amdgcn_exp2f(s[e] - mn); o[e] *= al; }
          const f32x8 lo = __builtin_shufflevector(s, s, 0, 1, 2, 3, 4, 5, 6, 7), hi = __builtin_shufflevector(s, s, 8, 9, 10, 11, 12, 13, 14, 15);
          o = __builtin_amdgcn_smfmac_f32_32x32x32_bf16(__builtin_bit_cast(bf16x8, v1), __builtin_bit_cast(bf16x16, lo), o, 0xDDDD, 0, 0);
          o = __builtin_amdgcn_smfmac_f32_32x32x32_bf16(__builtin_bit_cast(bf16x8, v2), __builtin_bit_cast(bf16x16, hi), o, 0xDDDD, 0, 0);
        }
        const float rl = 1.f / o[8];
        st16(v4u{pk2(o[0] * rl, o[1] * rl), pk2(o[2] * rl, o[3] * rl), pk2(o[4] * rl, o[5] * rl), pk2(o[6] * rl, o[7] * rl)}, rs, l16, oso);
        publish(rs, osf, tag, w, lane);
      }
    }
    {
      PHASE_VARS();
      if (layer == 0) {
        asm volatile(
          "v_and_b32 v72, 0xe0, %[l16]\n\tv_mov_b32 v69, 0x3f803f80\n\tv_cmp_eq_u32 vcc, 0, v72\n\tv_mov_b32 v76, 0\n\t"
          "v_mov_b32 v77, 0\n\tv_cndmask_b32 v68, 0, v69, vcc\n\tv_cmp_eq_u32 vcc, 32, v72\n\tv_mov_b32 v78, 0\n\t"
          "v_and_b32 v72, 16, %[l16]\n\tv_cndmask_b32 v70, 0, v69, vcc\n\tv_cmp_eq_u32 vcc, 0, v72\n\tv_mov_b32 v69, 0x4444\n\t"
          "v_mov_b32 v72, 0xeeee\n\tv_cndmask_b32 v72, v72, v69, vcc\n\tv_mov_b32 v69, v68\n\tv_mov_b32 v71, v70\n\t"
          "v_mov_b32 v79, 0\n\ts_nop 1\n\tv_smfmac_f32_16x16x64_bf16 v[76:79], v[68:71], v[128:135], v72\n\tv_smfmac_f32_16x16x64_bf16 v[76:79], v[68:71], v[136:143], v72\n\t"
          "v_smfmac_f32_16x16x64_bf16 v[76:79], v[68:71], v[144:151], v72\n\tv_smfmac_f32_16x16x64_bf16 v[76:79], v[68:71], v[152:159], v72\n\tv_smfmac_f32_16x16x64_bf16 v[76:79], v[68:71], v[160:167], v72\n\tv_smfmac_f32_16x16x64_bf16 v[76:79], v[68:71], v[168:175], v72\n\t"
          "v_smfmac_f32_16x16x64_bf16 v[76:79], v[68:71], v[176:183], v72\n\tv_smfmac_f32_16x16x64_bf16 v[76:79], v[68:71], v[184:191], v72\n\tv_smfmac_f32_16x16x64_bf16 v[76:79], v[68:71], v[192:199], v72\n\tv_smfmac_f32_16x16x64_bf16 v[76:79], v[68:71], v[200:207], v72\n\t"
          "v_smfmac_f32_16x16x64_bf16 v[76:79], v[68:71], v[208:215], v72\n\tv_smfmac_f32_16x16x64_bf16 v[76:79], v[68:71], v[216:223], v72\n\tv_smfmac_f32_16x16x64_bf16 v[76:79], v[68:71], v[224:231], v72\n\tv_smfmac_f32_16x16x64_bf16 v[76:79], v[68:71], v[232:239], v72\n\t"
          "v_smfmac_f32_16x16x64_bf16 v[76:79], v[68:71], v[240:247], v72\n\tv_smfmac_f32_16x16x64_bf16 v[76:79], v[68:71], v[248:255], v72\n\ts_nop 7\n\tv_add_f32 v76, v76, v77\n\t"
          "v_add_f32 v78, v78, v79\n\ts_mov_b64 exec, 0xffff\n\tds_write2_b32 %[vx], v76, v78 offset0:32 offset1:48\n\ts_mov_b64 exec, -1\n\t"
            :: [vx] "v"(ltab + tok * 4), [l16] "v"(l16) : "v68", "v69", "v70", "v71", "v72", "v73", "v74", "v75", "v76", "v77", "v78", "v79", "vcc", "memory");
      } else if (b == 0 && w == 1 && lane == 0) {
        u64 t = tag;
        asm volatile("" : "+s"(t));
        __builtin_amdgcn_raw_buffer_store_b64(__builtin_bit_cast(v2u, t), rs, 0, 0, 16);
      }
    }
    {
      PHASE_VARS();
      const int tt = w & 1, kq = w >> 1;
      u32 so = OFF_L0 + LO_O + ((2 * c + tt) * 32 + 8 * kq) * 1024;
      asm volatile(
          "s_nop 4\n\ts_waitcnt lgkmcnt(0)\n\tds_read_b128 v[40:43], %[vxr]\n\tds_read_b128 v[64:67], %[vwo] offset:0\n\t"
          "ds_read_b128 v[68:71], %[vwo] offset:32768\n\tds_read_b128 v[72:75], %[vwo] offset:1024\n\tds_read_b128 v[76:79], %[vwo] offset:33792\n\tds_read_b128 v[80:83], %[vwo] offset:2048\n\t"
          "ds_read_b128 v[84:87], %[vwo] offset:34816\n\tds_read_b128 v[88:91], %[vwo] offset:3072\n\tds_read_b128 v[92:95], %[vwo] offset:35840\n\tv_mov_b64 v[52:53], %[tag]\n\t"
          "v_mov_b64 v[54:55], %[tag]\n\tBPA%=:\n\tbuffer_load_dwordx2 v[32:33], %[vf], %[desc], 0 offen sc1\n\ts_waitcnt vmcnt(0)\n\t"
          "v_cmp_ne_u64 vcc, %[tag], v[32:33]\n\ts_cbranch_vccz BPB%=\n\ts_sub_u32 %[bud], %[bud], 1\n\ts_cbranch_scc0 BPA%=\n\t"
          "s_mov_b32 %[bud], 0\n\tBPB%=:\n\tbuffer_load_dwordx4 v[96:99], %[l16], %[desc], %[so] offen offset:0 sc1\n\tbuffer_load_dwordx4 v[100:103], %[l16], %[desc], %[so] offen offset:1024 sc1\n\t"
          "buffer_load_dwordx4 v[104:107], %[l16], %[desc], %[so] offen offset:2048 sc1\n\tbuffer_load_dwordx4 v[108:111], %[l16], %[desc], %[so] offen offset:3072 sc1\n\ts_add_u32 %[so], %[so], 4096\n\tbuffer_load_dwordx4 v[112:115], %[l16], %[desc], %[so] offen offset:0 sc1\n\t"
          "buffer_load_dwordx4 v[116:119], %[l16], %[desc], %[so] offen offset:1024 sc1\n\tbuffer_load_dwordx4 v[120:123], %[l16], %[desc], %[so] offen offset:2048 sc1\n\tbuffer_load_dwordx4 v[124:127], %[l16], %[desc], %[so] offen offset:3072 sc1\n\ts_waitcnt vmcnt(7)\n\t"
          "s_waitcnt lgkmcnt(7)\n\tv_mfma_f32_32x32x16_bf16 v[0:15], v[64:67], v[96:99], 0\n\tds_read_b128 v[64:67], %[vwo] offset:4096\n\ts_waitcnt lgkmcnt(7)\n\t"
          "v_mfma_f32_32x32x16_bf16 v[16:31], v[68:71], v[96:99], 0\n\tds_read_b128 v[68:71], %[vwo] offset:36864\n\ts_waitcnt vmcnt(6)\n\ts_waitcnt lgkmcnt(7)\n\t"
          "v_mfma_f32_32x32x16_bf16 v[0:15], v[72:75], v[100:103], v[0:15]\n\tds_read_b128 v[72:75], %[vwo] offset:5120\n\ts_waitcnt lgkmcnt(7)\n\tv_mfma_f32_32x32x16_bf16 v[16:31], v[76:79], v[100:103], v[16:31]\n\t"
          "ds_read_b128 v[76:79], %[vwo] offset:37888\n\ts_waitcnt vmcnt(5)\n\ts_waitcnt lgkmcnt(7)\n\tv_mfma_f32_32x32x16_bf16 v[0:15], v[80:83], v[104:107], v[0:15]\n\t"
          "ds_read_b128 v[80:83], %[vwo] offset:6144\n\ts_waitcnt lgkmcnt(7)\n\tv_mfma_f32_32x32x16_bf16 v[16:31], v[84:87], v[104:107], v[16:31]\n\tds_read_b128 v[84:87], %[vwo] offset:38912\n\t"
          "s_waitcnt vmcnt(4)\n\ts_waitcnt lgkmcnt(7)\n\tv_mfma_f32_32x32x16_bf16 v[0:15], v[88:91], v[108:111], v[0:15]\n\tds_read_b128 v[88:91], %[vwo] offset:7168\n\t"
          "s_waitcnt lgkmcnt(7)\n\tv_mfma_f32_32x32x16_bf16 v[16:31], v[92:95], v[108:111], v[16:31]\n\tds_read_b128 v[92:95], %[vwo] offset:39936\n\ts_waitcnt vmcnt(3)\n\t"
          "s_waitcnt lgkmcnt(7)\n\tv_mfma_f32_32x32x16_bf16 v[0:15], v[64:67], v[112:115], v[0:15]\n\ts_waitcnt lgkmcnt(6)\n\tv_mfma_f32_32x32x16_bf16 v[16:31], v[68:71], v[112:115], v[16:31]\n\t"
          "s_waitcnt vmcnt(2)\n\ts_waitcnt lgkmcnt(5)\n\tv_mfma_f32_32x32x16_bf16 v[0:15], v[72:75], v[116:119], v[0:15]\n\ts_waitcnt lgkmcnt(4)\n\t"
          "v_mfma_f32_32x32x16_bf16 v[16:31], v[76:79], v[116:119], v[16:31]\n\ts_waitcnt vmcnt(1)\n\ts_waitcnt lgkmcnt(3)\n\tv_mfma_f32_32x32x16_bf16 v[0:15], v[80:83], v[120:123], v[0:15]\n\t"
          "s_waitcnt lgkmcnt(2)\n\tv_mfma_f32_32x32x16_bf16 v[16:31], v[84:87], v[120:123], v[16:31]\n\ts_waitcnt vmcnt(0)\n\ts_waitcnt lgkmcnt(1)\n\t"
          "v_mfma_f32_32x32x16_bf16 v[0:15], v[88:91], v[124:127], v[0:15]\n\ts_waitcnt lgkmcnt(0)\n\tv_mfma_f32_32x32x16_bf16 v[16:31], v[92:95], v[124:127], v[16:31]\n\ts_nop 11\n\t"
          "ds_write_b128 %[vwr], v[0:3] offset:0\n\tds_write_b128 %[vwr], v[4:7] offset:1024\n\tds_write_b128 %[vwr], v[8:11] offset:16384\n\tds_write_b128 %[vwr], v[12:15] offset:17408\n\t"
          "ds_write_b128 %[vwr], v[16:19] offset:32768\n\tds_write_b128 %[vwr], v[20:23] offset:33792\n\tds_write_b128 %[vwr], v[24:27] offset:49152\n\tds_write_b128 %[vwr], v[28:31] offset:50176\n\t"
          "s_waitcnt lgkmcnt(0)\n\ts_barrier\n\tds_read_b128 v[0:3], %[vrd] offset:0\n\tds_read_b128 v[4:7], %[vrd] offset:1024\n\t"
          "ds_read_b128 v[8:11], %[vrd] offset:2048\n\tds_read_b128 v[12:15], %[vrd] offset:3072\n\tds_read_b128 v[16:19], %[vrd] offset:4096\n\tds_read_b128 v[20:23], %[vrd] offset:5120\n\t"
          "ds_read_b128 v[24:27], %[vrd] offset:6144\n\tds_read_b128 v[28:31], %[vrd] offset:7168\n\tv_lshlrev_b32 v44, 16, v40\n\tv_and_b32 v45, 0xffff0000, v40\n\t"
          "v_lshlrev_b32 v46, 16, v41\n\tv_and_b32 v47, 0xffff0000, v41\n\tv_lshlrev_b32 v48, 16, v42\n\tv_and_b32 v49, 0xffff0000, v42\n\t"
          "v_lshlrev_b32 v50, 16, v43\n\tv_and_b32 v51, 0xffff0000, v43\n\ts_waitcnt lgkmcnt(4)\n\tv_pk_add_f32 v[0:1], v[0:1], v[8:9]\n\t"
          "v_pk_add_f32 v[2:3], v[2:3], v[10:11]\n\tv_pk_add_f32 v[4:5], v[4:5], v[12:13]\n\tv_pk_add_f32 v[6:7], v[6:7], v[14:15]\n\ts_waitcnt lgkmcnt(0)\n\t"
          "v_pk_add_f32 v[16:17], v[16:17], v[24:25]\n\tv_pk_add_f32 v[18:19], v[18:19], v[26:27]\n\tv_pk_add_f32 v[20:21], v[20:21], v[28:29]\n\tv_pk_add_f32 v[22:23], v[22:23], v[30:31]\n\t"
          "v_pk_add_f32 v[0:1], v[0:1], v[16:17]\n\tv_pk_add_f32 v[2:3], v[2:3], v[18:19]\n\tv_pk_add_f32 v[4:5], v[4:5], v[20:21]\n\tv_pk_add_f32 v[6:7], v[6:7], v[22:23]\n\t"
          "v_pk_add_f32 v[44:45], v[44:45], v[0:1]\n\tv_pk_add_f32 v[46:47], v[46:47], v[2:3]\n\tv_pk_add_f32 v[48:49], v[48:49], v[4:5]\n\tv_pk_add_f32 v[50:51], v[50:51], v[6:7]\n\t"
          "v_cvt_pk_bf16_f32 v40, v44, v45\n\tv_cvt_pk_bf16_f32 v41, v46, v47\n\tv_cvt_pk_bf16_f32 v42, v48, v49\n\tv_cvt_pk_bf16_f32 v43, v50, v51\n\t"
          "buffer_store_dwordx4 v[40:43], %[l16], %[desc], %[sxo] offen sc1\n\tv_mov_b32 v44, 0\n\tv_mov_b32 v45, 0\n\tv_mov_b32 v46, 0\n\t"
          "v_mov_b32 v47, 0\n\tv_dot2c_f32_bf16 v44, 0x3f803f80, v40\n\tv_dot2c_f32_bf16 v45, v40, v40\n\tv_dot2c_f32_bf16 v46, 0x3f803f80, v41\n\t"
          "v_dot2c_f32_bf16 v47, v41, v41\n\tv_dot2c_f32_bf16 v44, 0x3f803f80, v42\n\tv_dot2c_f32_bf16 v45, v42, v42\n\tv_dot2c_f32_bf16 v46, 0x3f803f80, v43\n\t"
          "v_dot2c_f32_bf16 v47, v43, v43\n\tds_write_b128 %[vxr], v[40:43]\n\tv_lshrrev_b32 v48, 1, %[l16]\n\ts_nop 0\n\t"
          "v_pk_add_f32 v[44:45], v[44:45], v[46:47]\n\tv_mov_b64 v[46:47], v[44:45]\n\ts_nop 1\n\tv_permlane32_swap_b32 v44, v46\n\t"
          "v_permlane32_swap_b32 v45, v47\n\tv_pk_add_f32 v[44:45], v[44:45], v[46:47]\n\ts_mov_b32 exec_hi, 0\n\tbuffer_store_dwordx2 v[44:45], v48, %[desc], %[sso] offen sc1\n\t"
          "s_mov_b32 exec_hi, -1\n\tv_lshlrev_b32 v32, 3, %[l16]\n\ts_mov_b32 m0, %[mdu]\n\ts_waitcnt vmcnt(0) lgkmcnt(0)\n\t"
          "s_barrier\n\ts_cmp_lg_u32 %[w], 0\n\ts_cbranch_scc1 BQ%=\n\ts_mov_b64 exec, 0xff\n\t"
          "buffer_store_dwordx4 v[52:55], %[l16], %[desc], %[sf] offen sc1\n\ts_mov_b64 exec, -1\n\ts_nop 1\n\tBQ%=:\n\t"
          "s_nop 7\n\ts_mov_b64 exec, 0xff\n\tbuffer_load_dword v32, %[desc], %[sxo] offen sc1 lds\n\ts_mov_b64 exec, 0x3\n\t"
          "buffer_load_dword v32, %[desc], %[sso] offen sc1 lds\n\ts_mov_b64 exec, -1\n\t"
          : [so] "+s"(so), [bud] "+s"(bud)
          : [l16] "v"(l16), [vwo] "v"(lds0 + L_WO + kq * 8192 + l16), [vxr] "v"(lds0 + L_XRES + w * 1024 + l16), [vf] "v"(flag_off(SL_O + sl, c * 8 + 2 * kq + (lane & 1))),
            [vwr] "v"(lds0 + (tt * 4 + kq) * 2048 + l16), [vrd] "v"(lds0 + w * 8192 + l16),
            [desc] "s"(desc), [w] "s"(w), [tag] "s"(tag), [sxo] "s"(OFF_L0 + LO_X2 + ((2 * c + tt) * 32 + 4 * j + kq) * 1024),
            [sso] "s"(OFF_L0 + LO_S2 + (c * 8 + j) * 2048 + w * 256), [sf] "s"(flag_off(SL_X2 + sl, c * 8 + j)), [mdu] "s"(US(lds0 + L_DUMMY))
          : "v0", "v1", "v2", "v3", "v4", "v5", "v6", "v7", "v8", "v9", "v10", "v11", "v12", "v13", "v14", "v15", "v16", "v17", "v18", "v19", "v20", "v21", "v22", "v23", "v24", "v25", "v26", "v27", "v28", "v29", "v30", "v31", "v32", "v33", "v34", "v35", "v36", "v37", "v38", "v39", "v40", "v41", "v42", "v43", "v44", "v45", "v46", "v47", "v48", "v49", "v50", "v51", "v52", "v53", "v54", "v55", "v64", "v65", "v66", "v67", "v68", "v69", "v70", "v71", "v72", "v73", "v74", "v75", "v76", "v77", "v78", "v79", "v80", "v81", "v82", "v83", "v84", "v85", "v86", "v87", "v88", "v89", "v90", "v91", "v92", "v93", "v94", "v95", "v96", "v97", "v98", "v99", "v100", "v101", "v102", "v103", "v104", "v105", "v106", "v107", "v108", "v109", "v110", "v111", "v112", "v113", "v114", "v115", "v116", "v117", "v118", "v119", "v120", "v121", "v122", "v123", "v124", "v125", "v126", "v127", "scc", "vcc", "memory");
    }
    f32x16 hp;
    {
      PHASE_VARS();
      const int mm = (w + j) & 7, t0 = (w + 1) >> 3, ntl = w < 6 ? w : 6;
      u32 sx = OFF_L0 + LO_X2 + 2 * c * 32768 + mm * 4096;
      u32 sw = OFF_W2 + (j * 224 + (w < 5 ? w * 32 : w < 7 ? 24 + w * 26 : 202)) * 1024, sq = OFF_WQKV + qkv_tile(w, j) * 32768;
      asm volatile(
          "s_nop 4\n\ts_waitcnt lgkmcnt(0)\n\ts_cmp_ge_u32 %[w], 6\n\ts_cbranch_scc1 M0%=\n\t"
          "ds_read_b128 v[96:99], %[vx0] offset:0\n\tds_read_b128 v[100:103], %[vx0] offset:1024\n\tds_read_b128 v[104:107], %[vx0] offset:2048\n\tds_read_b128 v[108:111], %[vx0] offset:3072\n\t"
          "ds_read_b128 v[112:115], %[vx0] offset:4096\n\tds_read_b128 v[116:119], %[vx0] offset:5120\n\tds_read_b128 v[120:123], %[vx0] offset:6144\n\tds_read_b128 v[124:127], %[vx0] offset:7168\n\t"
          "s_waitcnt lgkmcnt(7)\n\tv_mfma_f32_32x32x16_bf16 v[0:15], v[128:131], v[96:99], 0\n\ts_waitcnt lgkmcnt(6)\n\tv_mfma_f32_32x32x16_bf16 v[16:31], v[128:131], v[100:103], 0\n\t"
          "buffer_load_dwordx4 v[128:131], %[l16], %[desc], %[sw] offen offset:0 sc1\n\ts_waitcnt lgkmcnt(5)\n\tv_mfma_f32_32x32x16_bf16 v[0:15], v[132:135], v[104:107], v[0:15]\n\ts_waitcnt lgkmcnt(4)\n\t"
          "v_mfma_f32_32x32x16_bf16 v[16:31], v[132:135], v[108:111], v[16:31]\n\tbuffer_load_dwordx4 v[132:135], %[l16], %[desc], %[sw] offen offset:1024 sc1\n\ts_waitcnt lgkmcnt(3)\n\tv_mfma_f32_32x32x16_bf16 v[0:15], v[136:139], v[112:115], v[0:15]\n\t"
          "s_waitcnt lgkmcnt(2)\n\tv_mfma_f32_32x32x16_bf16 v[16:31], v[136:139], v[116:119], v[16:31]\n\tbuffer_load_dwordx4 v[136:139], %[l16], %[desc], %[sw] offen offset:2048 sc1\n\ts_waitcnt lgkmcnt(1)\n\t"
          "v_mfma_f32_32x32x16_bf16 v[0:15], v[140:143], v[120:123], v[0:15]\n\ts_waitcnt lgkmcnt(0)\n\tv_mfma_f32_32x32x16_bf16 v[16:31], v[140:143], v[124:127], v[16:31]\n\tbuffer_load_dwordx4 v[140:143], %[l16], %[desc], %[sw] offen offset:3072 sc1\n\t"
          "s_add_u32 %[sw], %[sw], 4096\n\ts_branch M1%=\n\tM0%=:\n\tds_read_b128 v[96:99], %[vx0] offset:0\n\t"
          "ds_read_b128 v[100:103], %[vx0] offset:2048\n\tds_read_b128 v[104:107], %[vx0] offset:4096\n\tds_read_b128 v[108:111], %[vx0] offset:6144\n\ts_waitcnt lgkmcnt(3)\n\t"
          "v_mfma_f32_32x32x16_bf16 v[0:15], v[128:131], v[96:99], 0\n\tbuffer_load_dwordx4 v[128:131], %[l16], %[desc], %[sw] offen offset:0 sc1\n\ts_waitcnt lgkmcnt(2)\n\tv_mfma_f32_32x32x16_bf16 v[0:15], v[132:135], v[100:103], v[0:15]\n\t"
          "buffer_load_dwordx4 v[132:135], %[l16], %[desc], %[sw] offen offset:1024 sc1\n\ts_waitcnt lgkmcnt(1)\n\tv_mfma_f32_32x32x16_bf16 v[0:15], v[136:139], v[104:107], v[0:15]\n\tbuffer_load_dwordx4 v[136:139], %[l16], %[desc], %[sw] offen offset:2048 sc1\n\t"
          "s_waitcnt lgkmcnt(0)\n\tv_mfma_f32_32x32x16_bf16 v[0:15], v[140:143], v[108:111], v[0:15]\n\tbuffer_load_dwordx4 v[140:143], %[l16], %[desc], %[sw] offen offset:3072 sc1\n\ts_add_u32 %[sw], %[sw], 4096\n\t"
          "M1%=:\n\ts_mov_b32 m0, %[ma]\n\tv_lshrrev_b32 v76, 1, %[l16]\n\ts_cmp_lt_u32 %[w], 4\n\t"
          "s_cbranch_scc1 MPO%=\n\tMPO%=:\n\tMPA%=:\n\tbuffer_load_dwordx2 v[68:69], %[vf], %[desc], 0 offen sc1\n\t"
          "s_waitcnt vmcnt(0)\n\tv_cmp_ne_u64 vcc, %[tag], v[68:69]\n\ts_cbranch_vccz MPB%=\n\ts_sub_u32 %[bud], %[bud], 1\n\t"
          "s_cbranch_scc0 MPA%=\n\ts_mov_b32 %[bud], 0\n\tMPB%=:\n\ts_cmp_eq_u32 %[w], 0\n\t"
          "s_cbranch_scc1 MPS%=\n\tbuffer_load_dwordx4 %[l16], %[desc], %[sx] offen offset:0 sc1 lds\n\tbuffer_load_dwordx4 %[l16], %[desc], %[sx] offen offset:1024 sc1 lds\n\tbuffer_load_dwordx4 %[l16], %[desc], %[sx] offen offset:2048 sc1 lds\n\t"
          "buffer_load_dwordx4 %[l16], %[desc], %[sx] offen offset:3072 sc1 lds\n\ts_add_u32 m0, m0, 32768\n\ts_add_u32 %[sx], %[sx], 32768\n\tbuffer_load_dwordx4 %[l16], %[desc], %[sx] offen offset:0 sc1 lds\n\t"
          "buffer_load_dwordx4 %[l16], %[desc], %[sx] offen offset:1024 sc1 lds\n\tbuffer_load_dwordx4 %[l16], %[desc], %[sx] offen offset:2048 sc1 lds\n\tbuffer_load_dwordx4 %[l16], %[desc], %[sx] offen offset:3072 sc1 lds\n\tMPS%=:\n\t"
          "buffer_load_dwordx2 v[68:69], v76, %[desc], %[ss] offen offset:0 sc1\n\tbuffer_load_dwordx2 v[70:71], v76, %[desc], %[ss] offen offset:512 sc1\n\tbuffer_load_dwordx2 v[72:73], v76, %[desc], %[ss] offen offset:1024 sc1\n\tbuffer_load_dwordx2 v[74:75], v76, %[desc], %[ss] offen offset:1536 sc1\n\t"
          "s_cmp_lt_u32 %[w], 4\n\ts_cbranch_scc1 MPR%=\n\ts_barrier\n\tMPR%=:\n\t"
          "s_waitcnt vmcnt(0)\n\tv_pk_add_f32 v[68:69], v[68:69], v[70:71]\n\tv_pk_add_f32 v[72:73], v[72:73], v[74:75]\n\tv_pk_add_f32 v[68:69], v[68:69], v[72:73]\n\t"
          "ds_write_b64 %[vs], v[68:69]\n\ts_waitcnt lgkmcnt(0)\n\ts_barrier\n\ts_cmp_ge_u32 %[w], 4\n\t"
          "s_cbranch_scc1 MY%=\n\ts_setprio 2\n\tds_read_b128 v[96:99], %[vr] offset:4096\n\tds_read_b128 v[100:103], %[vr] offset:36864\n\t"
          "ds_read_b128 v[104:107], %[vr] offset:5120\n\tds_read_b128 v[108:111], %[vr] offset:37888\n\tds_read_b128 v[112:115], %[vr] offset:6144\n\tds_read_b128 v[116:119], %[vr] offset:38912\n\t"
          "ds_read_b128 v[120:123], %[vr] offset:7168\n\tds_read_b128 v[124:127], %[vr] offset:39936\n\ts_waitcnt lgkmcnt(7)\n\tv_mfma_f32_32x32x16_bf16 v[0:15], v[144:147], v[96:99], v[0:15]\n\t"
          "ds_read_b128 v[96:99], %[vr] offset:8192\n\ts_waitcnt lgkmcnt(7)\n\tv_mfma_f32_32x32x16_bf16 v[16:31], v[144:147], v[100:103], v[16:31]\n\tds_read_b128 v[100:103], %[vr] offset:40960\n\t"
          "s_waitcnt lgkmcnt(7)\n\tv_mfma_f32_32x32x16_bf16 v[0:15], v[148:151], v[104:107], v[0:15]\n\tds_read_b128 v[104:107], %[vr] offset:9216\n\ts_waitcnt lgkmcnt(7)\n\t"
          "v_mfma_f32_32x32x16_bf16 v[16:31], v[148:151], v[108:111], v[16:31]\n\tds_read_b128 v[108:111], %[vr] offset:41984\n\ts_waitcnt lgkmcnt(7)\n\tv_mfma_f32_32x32x16_bf16 v[0:15], v[152:155], v[112:115], v[0:15]\n\t"
          "ds_read_b128 v[112:115], %[vr] offset:10240\n\ts_waitcnt lgkmcnt(7)\n\tv_mfma_f32_32x32x16_bf16 v[16:31], v[152:155], v[116:119], v[16:31]\n\tds_read_b128 v[116:119], %[vr] offset:43008\n\t"
          "s_waitcnt lgkmcnt(7)\n\tv_mfma_f32_32x32x16_bf16 v[0:15], v[156:159], v[120:123], v[0:15]\n\tds_read_b128 v[120:123], %[vr] offset:11264\n\ts_waitcnt lgkmcnt(7)\n\t"
          "v_mfma_f32_32x32x16_bf16 v[16:31], v[156:159], v[124:127], v[16:31]\n\tds_read_b128 v[124:127], %[vr] offset:44032\n\ts_waitcnt lgkmcnt(7)\n\tv_mfma_f32_32x32x16_bf16 v[0:15], v[160:163], v[96:99], v[0:15]\n\t"
          "ds_read_b128 v[96:99], %[vr] offset:12288\n\ts_waitcnt lgkmcnt(7)\n\tv_mfma_f32_32x32x16_bf16 v[16:31], v[160:163], v[100:103], v[16:31]\n\tds_read_b128 v[100:103], %[vr] offset:45056\n\t"
          "s_waitcnt lgkmcnt(7)\n\tv_mfma_f32_32x32x16_bf16 v[0:15], v[164:167], v[104:107], v[0:15]\n\tds_read_b128 v[104:107], %[vr] offset:13312\n\ts_waitcnt lgkmcnt(7)\n\t"
          "v_mfma_f32_32x32x16_bf16 v[16:31], v[164:167], v[108:111], v[16:31]\n\tds_read_b128 v[108:111], %[vr] offset:46080\n\ts_waitcnt lgkmcnt(7)\n\tv_mfma_f32_32x32x16_bf16 v[0:15], v[168:171], v[112:115], v[0:15]\n\t"
          "ds_read_b128 v[112:115], %[vr] offset:14336\n\ts_waitcnt lgkmcnt(7)\n\tv_mfma_f32_32x32x16_bf16 v[16:31], v[168:171], v[116:119], v[16:31]\n\tds_read_b128 v[116:119], %[vr] offset:47104\n\t"
          "s_waitcnt lgkmcnt(7)\n\tv_mfma_f32_32x32x16_bf16 v[0:15], v[172:175], v[120:123], v[0:15]\n\tds_read_b128 v[120:123], %[vr] offset:15360\n\ts_waitcnt lgkmcnt(7)\n\t"
          "v_mfma_f32_32x32x16_bf16 v[16:31], v[172:175], v[124:127], v[16:31]\n\tds_read_b128 v[124:127], %[vr] offset:48128\n\ts_waitcnt lgkmcnt(7)\n\tv_mfma_f32_32x32x16_bf16 v[0:15], v[176:179], v[96:99], v[0:15]\n\t"
          "s_barrier\n\tds_read_b128 v[96:99], %[vr] offset:16384\n\ts_waitcnt lgkmcnt(7)\n\tv_mfma_f32_32x32x16_bf16 v[16:31], v[176:179], v[100:103], v[16:31]\n\t"
          "ds_read_b128 v[100:103], %[vr] offset:49152\n\tbuffer_load_dwordx4 v[144:147], %[l16], %[desc], %[sw] offen offset:0 sc1\n\tbuffer_load_dwordx4 v[148:151], %[l16], %[desc], %[sw] offen offset:1024 sc1\n\ts_waitcnt lgkmcnt(7)\n\t"
          "v_mfma_f32_32x32x16_bf16 v[0:15], v[180:183], v[104:107], v[0:15]\n\tds_read_b128 v[104:107], %[vr] offset:17408\n\tds_read_b128 v[32:35], %[vst] offset:0\n\ts_waitcnt lgkmcnt(8)\n\t"
          "v_mfma_f32_32x32x16_bf16 v[16:31], v[180:183], v[108:111], v[16:31]\n\tds_read_b128 v[108:111], %[vr] offset:50176\n\tbuffer_load_dwordx4 v[152:155], %[l16], %[desc], %[sw] offen offset:2048 sc1\n\tbuffer_load_dwordx4 v[156:159], %[l16], %[desc], %[sw] offen offset:3072 sc1\n\t"
          "s_add_u32 %[sw], %[sw], 4096\n\tds_read_b128 v[36:39], %[vst] offset:512\n\ts_waitcnt lgkmcnt(9)\n\tv_mfma_f32_32x32x16_bf16 v[0:15], v[184:187], v[112:115], v[0:15]\n\t"
          "ds_read_b128 v[112:115], %[vr] offset:18432\n\tds_read_b128 v[40:43], %[vst] offset:1024\n\ts_waitcnt lgkmcnt(10)\n\tv_mfma_f32_32x32x16_bf16 v[16:31], v[184:187], v[116:119], v[16:31]\n\t"
          "ds_read_b128 v[116:119], %[vr] offset:51200\n\tbuffer_load_dwordx4 v[160:163], %[l16], %[desc], %[sw] offen offset:0 sc1\n\tbuffer_load_dwordx4 v[164:167], %[l16], %[desc], %[sw] offen offset:1024 sc1\n\tds_read_b128 v[44:47], %[vst] offset:1536\n\t"
          "s_waitcnt lgkmcnt(11)\n\tv_mfma_f32_32x32x16_bf16 v[0:15], v[188:191], v[120:123], v[0:15]\n\tds_read_b128 v[120:123], %[vr] offset:19456\n\tds_read_b128 v[48:51], %[vst] offset:2048\n\t"
          "s_waitcnt lgkmcnt(12)\n\tv_mfma_f32_32x32x16_bf16 v[16:31], v[188:191], v[124:127], v[16:31]\n\tds_read_b128 v[124:127], %[vr] offset:52224\n\tbuffer_load_dwordx4 v[168:171], %[l16], %[desc], %[sw] offen offset:2048 sc1\n\t"
          "buffer_load_dwordx4 v[172:175], %[l16], %[desc], %[sw] offen offset:3072 sc1\n\ts_add_u32 %[sw], %[sw], 4096\n\tds_read_b128 v[52:55], %[vst] offset:2560\n\ts_waitcnt lgkmcnt(13)\n\t"
          "v_mfma_f32_32x32x16_bf16 v[0:15], v[192:195], v[96:99], v[0:15]\n\tds_read_b128 v[96:99], %[vr] offset:20480\n\ts_waitcnt lgkmcnt(13)\n\tds_read_b128 v[56:59], %[vst] offset:3072\n\t"
          "v_mfma_f32_32x32x16_bf16 v[16:31], v[192:195], v[100:103], v[16:31]\n\tds_read_b128 v[100:103], %[vr] offset:53248\n\tbuffer_load_dwordx4 v[176:179], %[l16], %[desc], %[sw] offen offset:0 sc1\n\tbuffer_load_dwordx4 v[180:183], %[l16], %[desc], %[sw] offen offset:1024 sc1\n\t"
          "s_waitcnt lgkmcnt(13)\n\tds_read_b128 v[60:63], %[vst] offset:3584\n\ts_waitcnt lgkmcnt(12)\n\tv_add_f32 v32, v32, v36\n\t"
          "v_mfma_f32_32x32x16_bf16 v[0:15], v[196:199], v[104:107], v[0:15]\n\tds_read_b128 v[104:107], %[vr] offset:21504\n\tv_add_f32 v33, v33, v37\n\tv_add_f32 v34, v34, v38\n\t"
          "v_mfma_f32_32x32x16_bf16 v[16:31], v[196:199], v[108:111], v[16:31]\n\tds_read_b128 v[108:111], %[vr] offset:54272\n\tbuffer_load_dwordx4 v[184:187], %[l16], %[desc], %[sw] offen offset:2048 sc1\n\tbuffer_load_dwordx4 v[188:191], %[l16], %[desc], %[sw] offen offset:3072 sc1\n\t"
          "s_add_u32 %[sw], %[sw], 4096\n\tv_add_f32 v35, v35, v39\n\ts_waitcnt lgkmcnt(10)\n\tv_add_f32 v40, v40, v44\n\t"
          "v_mfma_f32_32x32x16_bf16 v[0:15], v[200:203], v[112:115], v[0:15]\n\tds_read_b128 v[112:115], %[vr] offset:22528\n\tv_add_f32 v41, v41, v45\n\tv_add_f32 v42, v42, v46\n\t"
          "v_mfma_f32_32x32x16_bf16 v[16:31], v[200:203], v[116:119], v[16:31]\n\tds_read_b128 v[116:119], %[vr] offset:55296\n\tbuffer_load_dwordx4 v[192:195], %[l16], %[desc], %[sw] offen offset:0 sc1\n\tbuffer_load_dwordx4 v[196:199], %[l16], %[desc], %[sw] offen offset:1024 sc1\n\t"
          "v_add_f32 v43, v43, v47\n\ts_waitcnt lgkmcnt(8)\n\tv_add_f32 v48, v48, v52\n\tv_mfma_f32_32x32x16_bf16 v[0:15], v[204:207], v[120:123], v[0:15]\n\t"
          "ds_read_b128 v[120:123], %[vr] offset:23552\n\tv_add_f32 v49, v49, v53\n\tv_add_f32 v50, v50, v54\n\tv_mfma_f32_32x32x16_bf16 v[16:31], v[204:207], v[124:127], v[16:31]\n\t"
          "ds_read_b128 v[124:127], %[vr] offset:56320\n\tbuffer_load_dwordx4 v[200:203], %[l16], %[desc], %[sw] offen offset:2048 sc1\n\tbuffer_load_dwordx4 v[204:207], %[l16], %[desc], %[sw] offen offset:3072 sc1\n\ts_add_u32 %[sw], %[sw], 4096\n\t"
          "v_add_f32 v51, v51, v55\n\ts_waitcnt lgkmcnt(6)\n\tv_add_f32 v56, v56, v60\n\tv_mfma_f32_32x32x16_bf16 v[0:15], v[208:211], v[96:99], v[0:15]\n\t"
          "ds_read_b128 v[96:99], %[vr] offset:24576\n\tv_add_f32 v57, v57, v61\n\tv_add_f32 v58, v58, v62\n\tv_mfma_f32_32x32x16_bf16 v[16:31], v[208:211], v[100:103], v[16:31]\n\t"
          "ds_read_b128 v[100:103], %[vr] offset:57344\n\tbuffer_load_dwordx4 v[208:211], %[l16], %[desc], %[sw] offen offset:0 sc1\n\tv_add_f32 v59, v59, v63\n\tv_add_f32 v32, v32, v40\n\t"
          "s_waitcnt lgkmcnt(7)\n\tv_mfma_f32_32x32x16_bf16 v[0:15], v[212:215], v[104:107], v[0:15]\n\tds_read_b128 v[104:107], %[vr] offset:25600\n\tv_add_f32 v33, v33, v41\n\t"
          "v_add_f32 v34, v34, v42\n\ts_waitcnt lgkmcnt(7)\n\tv_mfma_f32_32x32x16_bf16 v[16:31], v[212:215], v[108:111], v[16:31]\n\tds_read_b128 v[108:111], %[vr] offset:58368\n\t"
          "buffer_load_dwordx4 v[212:215], %[l16], %[desc], %[sw] offen offset:1024 sc1\n\tv_add_f32 v35, v35, v43\n\tv_add_f32 v48, v48, v56\n\ts_waitcnt lgkmcnt(7)\n\t"
          "v_mfma_f32_32x32x16_bf16 v[0:15], v[216:219], v[112:115], v[0:15]\n\tds_read_b128 v[112:115], %[vr] offset:26624\n\tv_add_f32 v49, v49, v57\n\tv_add_f32 v50, v50, v58\n\t"
          "s_waitcnt lgkmcnt(7)\n\tv_mfma_f32_32x32x16_bf16 v[16:31], v[216:219], v[116:119], v[16:31]\n\tds_read_b128 v[116:119], %[vr] offset:59392\n\tbuffer_load_dwordx4 v[216:219], %[l16], %[desc], %[sw] offen offset:2048 sc1\n\t"
          "v_add_f32 v51, v51, v59\n\tv_add_f32 v32, v32, v48\n\ts_waitcnt lgkmcnt(7)\n\tv_mfma_f32_32x32x16_bf16 v[0:15], v[220:223], v[120:123], v[0:15]\n\t"
          "ds_read_b128 v[120:123], %[vr] offset:27648\n\tv_add_f32 v33, v33, v49\n\tv_add_f32 v34, v34, v50\n\ts_waitcnt lgkmcnt(7)\n\t"
          "v_mfma_f32_32x32x16_bf16 v[16:31], v[220:223], v[124:127], v[16:31]\n\tds_read_b128 v[124:127], %[vr] offset:60416\n\tbuffer_load_dwordx4 v[220:223], %[l16], %[desc], %[sw] offen offset:3072 sc1\n\ts_add_u32 %[sw], %[sw], 4096\n\t"
          "v_add_f32 v35, v35, v51\n\tv_mul_f32 v64, 0xbb000000, v32\n\ts_waitcnt lgkmcnt(7)\n\tv_mfma_f32_32x32x16_bf16 v[0:15], v[224:227], v[96:99], v[0:15]\n\t"
          "ds_read_b128 v[96:99], %[vr] offset:28672\n\tv_mul_f32 v66, 0xbb000000, v34\n\tv_mul_f32 v68, 0x3b000000, v33\n\ts_waitcnt lgkmcnt(7)\n\t"
          "v_mfma_f32_32x32x16_bf16 v[16:31], v[224:227], v[100:103], v[16:31]\n\tds_read_b128 v[100:103], %[vr] offset:61440\n\tv_mul_f32 v69, 0x3b000000, v35\n\tv_fma_f32 v68, -v64, v64, v68\n\t"
          "s_waitcnt lgkmcnt(7)\n\tv_mfma_f32_32x32x16_bf16 v[0:15], v[228:231], v[104:107], v[0:15]\n\tds_read_b128 v[104:107], %[vr] offset:29696\n\tv_fma_f32 v69, -v66, v66, v69\n\t"
          "v_max_f32 v68, 0, v68\n\ts_waitcnt lgkmcnt(7)\n\tv_mfma_f32_32x32x16_bf16 v[16:31], v[228:231], v[108:111], v[16:31]\n\tds_read_b128 v[108:111], %[vr] offset:62464\n\t"
          "v_max_f32 v69, 0, v69\n\tv_add_f32 v70, 0x3727c5ac, v68\n\ts_waitcnt lgkmcnt(7)\n\tv_mfma_f32_32x32x16_bf16 v[0:15], v[232:235], v[112:115], v[0:15]\n\t"
          "ds_read_b128 v[112:115], %[vr] offset:30720\n\tv_add_f32 v71, 0x3727c5ac, v69\n\ts_waitcnt lgkmcnt(7)\n\tv_mfma_f32_32x32x16_bf16 v[16:31], v[232:235], v[116:119], v[16:31]\n\t"
          "ds_read_b128 v[116:119], %[vr] offset:63488\n\tv_rsq_f32 v65, v70\n\ts_waitcnt lgkmcnt(7)\n\tv_mfma_f32_32x32x16_bf16 v[0:15], v[236:239], v[120:123], v[0:15]\n\t"
          "ds_read_b128 v[120:123], %[vr] offset:31744\n\tv_rsq_f32 v67, v71\n\ts_waitcnt lgkmcnt(7)\n\tv_mfma_f32_32x32x16_bf16 v[16:31], v[236:239], v[124:127], v[16:31]\n\t"
          "ds_read_b128 v[124:127], %[vr] offset:64512\n\ts_nop 0\n\tv_mul_f32 v68, v68, v65\n\tv_mul_f32 v69, v69, v67\n\t"
          "s_waitcnt lgkmcnt(7)\n\tv_mfma_f32_32x32x16_bf16 v[0:15], v[240:243], v[96:99], v[0:15]\n\tv_fmaak_f32 v68, v68, v65, 0x3727c5ac\n\tv_fmaak_f32 v69, v69, v67, 0x3727c5ac\n\t"
          "s_waitcnt lgkmcnt(6)\n\tv_mfma_f32_32x32x16_bf16 v[16:31], v[240:243], v[100:103], v[16:31]\n\tv_rsq_f32 v68, v68\n\ts_waitcnt lgkmcnt(5)\n\t"
          "v_mfma_f32_32x32x16_bf16 v[0:15], v[244:247], v[104:107], v[0:15]\n\tv_rsq_f32 v69, v69\n\ts_waitcnt lgkmcnt(4)\n\tv_mfma_f32_32x32x16_bf16 v[16:31], v[244:247], v[108:111], v[16:31]\n\t"
          "v_mul_f32 v65, 0x3eeff558, v65\n\tv_mul_f32 v67, 0x3eeff558, v67\n\ts_waitcnt lgkmcnt(3)\n\tv_mfma_f32_32x32x16_bf16 v[0:15], v[248:251], v[112:115], v[0:15]\n\t"
          "v_mul_f32 v65, v65, v68\n\tv_mul_f32 v67, v67, v69\n\ts_waitcnt lgkmcnt(2)\n\tv_mfma_f32_32x32x16_bf16 v[16:31], v[248:251], v[116:119], v[16:31]\n\t"
          "v_mov_b32 v84, 0xc09d30fc\n\tv_mov_b32 v85, 0x3eeff558\n\ts_waitcnt lgkmcnt(1)\n\tv_mfma_f32_32x32x16_bf16 v[0:15], v[252:255], v[120:123], v[0:15]\n\t"
          "ds_read_b128 v[32:35], %[vtab] offset:128\n\ts_waitcnt lgkmcnt(1)\n\tv_mfma_f32_32x32x16_bf16 v[16:31], v[252:255], v[124:127], v[16:31]\n\tds_read_b128 v[36:39], %[vtab] offset:160\n\t"
          "ds_read_b128 v[40:43], %[vtab] offset:192\n\tds_read_b128 v[44:47], %[vtab] offset:224\n\tds_read_b128 v[48:51], %[vtab] offset:256\n\tds_read_b128 v[52:55], %[vtab] offset:288\n\t"
          "ds_read_b128 v[56:59], %[vtab] offset:320\n\tds_read_b128 v[60:63], %[vtab] offset:352\n\ts_waitcnt lgkmcnt(0)\n\ts_setprio 2\n\t"
          "s_nop 10\n\tv_fmac_f32 v0, v64, v32\n\tv_fmac_f32 v1, v64, v33\n\tv_fmac_f32 v2, v64, v34\n\t"
          "v_fmac_f32 v3, v64, v35\n\tv_fmac_f32 v4, v64, v36\n\tv_fmac_f32 v5, v64, v37\n\tv_fmac_f32 v6, v64, v38\n\t"
          "v_fmac_f32 v7, v64, v39\n\tv_fmac_f32 v8, v64, v40\n\tv_fmac_f32 v9, v64, v41\n\tv_fmac_f32 v10, v64, v42\n\t"
          "v_fmac_f32 v11, v64, v43\n\tv_fmac_f32 v12, v64, v44\n\tv_fmac_f32 v13, v64, v45\n\tv_fmac_f32 v14, v64, v46\n\t"
          "v_fmac_f32 v15, v64, v47\n\tv_fma_f32 v0, v65, v0, v48\n\tv_fma_f32 v1, v65, v1, v49\n\tv_fma_f32 v2, v65, v2, v50\n\t"
          "v_fma_f32 v3, v65, v3, v51\n\tv_fma_f32 v4, v65, v4, v52\n\tv_fma_f32 v5, v65, v5, v53\n\tv_fma_f32 v6, v65, v6, v54\n\t"
          "v_fma_f32 v7, v65, v7, v55\n\tv_fma_f32 v8, v65, v8, v56\n\tv_fma_f32 v9, v65, v9, v57\n\tv_fma_f32 v10, v65, v10, v58\n\t"
          "v_fma_f32 v11, v65, v11, v59\n\tv_fma_f32 v12, v65, v12, v60\n\tv_fma_f32 v13, v65, v13, v61\n\tv_fma_f32 v14, v65, v14, v62\n\t"
          "v_fma_f32 v15, v65, v15, v63\n\tv_fma_f32 v68, -v0, v0, v84\n\tv_fma_f32 v69, -v1, v1, v84\n\tv_fma_f32 v70, -v2, v2, v84\n\t"
          "v_fma_f32 v71, -v3, v3, v84\n\tv_fma_f32 v72, -v4, v4, v84\n\tv_fma_f32 v73, -v5, v5, v84\n\tv_fma_f32 v74, -v6, v6, v84\n\t"
          "v_fma_f32 v75, -v7, v7, v84\n\tv_fma_f32 v76, -v8, v8, v84\n\tv_fma_f32 v77, -v9, v9, v84\n\tv_fma_f32 v78, -v10, v10, v84\n\t"
          "v_fma_f32 v79, -v11, v11, v84\n\tv_fma_f32 v80, -v12, v12, v84\n\tv_fma_f32 v81, -v13, v13, v84\n\tv_fma_f32 v82, -v14, v14, v84\n\t"
          "v_fma_f32 v83, -v15, v15, v84\n\tv_mul_f32 v68, v68, v0\n\tv_mul_f32 v69, v69, v1\n\tv_mul_f32 v70, v70, v2\n\t"
          "v_mul_f32 v71, v71, v3\n\tv_mul_f32 v72, v72, v4\n\tv_mul_f32 v73, v73, v5\n\tv_mul_f32 v74, v74, v6\n\t"
          "v_mul_f32 v75, v75, v7\n\tv_mul_f32 v76, v76, v8\n\tv_mul_f32 v77, v77, v9\n\tv_mul_f32 v78, v78, v10\n\t"
          "v_mul_f32 v79, v79, v11\n\tv_mul_f32 v80, v80, v12\n\tv_mul_f32 v81, v81, v13\n\tv_mul_f32 v82, v82, v14\n\t"
          "v_mul_f32 v83, v83, v15\n\tv_exp_f32 v68, v68\n\tv_exp_f32 v69, v69\n\tv_exp_f32 v70, v70\n\t"
          "v_exp_f32 v71, v71\n\tv_exp_f32 v72, v72\n\tv_exp_f32 v73, v73\n\tv_exp_f32 v74, v74\n\t"
          "v_exp_f32 v75, v75\n\tv_exp_f32 v76, v76\n\tv_exp_f32 v77, v77\n\tv_exp_f32 v78, v78\n\t"
          "v_exp_f32 v79, v79\n\tv_exp_f32 v80, v80\n\tv_exp_f32 v81, v81\n\tv_exp_f32 v82, v82\n\t"
          "v_exp_f32 v83, v83\n\tv_fma_f32 v68, v68, v85, v85\n\tv_fma_f32 v69, v69, v85, v85\n\tv_fma_f32 v70, v70, v85, v85\n\t"
          "v_fma_f32 v71, v71, v85, v85\n\tv_fma_f32 v72, v72, v85, v85\n\tv_fma_f32 v73, v73, v85, v85\n\tv_fma_f32 v74, v74, v85, v85\n\t"
          "v_fma_f32 v75, v75, v85, v85\n\tv_fma_f32 v76, v76, v85, v85\n\tv_fma_f32 v77, v77, v85, v85\n\tv_fma_f32 v78, v78, v85, v85\n\t"
          "v_fma_f32 v79, v79, v85, v85\n\tv_fma_f32 v80, v80, v85, v85\n\tv_fma_f32 v81, v81, v85, v85\n\tv_fma_f32 v82, v82, v85, v85\n\t"
          "v_fma_f32 v83, v83, v85, v85\n\tv_rcp_f32 v68, v68\n\tv_rcp_f32 v69, v69\n\tv_rcp_f32 v70, v70\n\t"
          "v_rcp_f32 v71, v71\n\tv_rcp_f32 v72, v72\n\tv_rcp_f32 v73, v73\n\tv_rcp_f32 v74, v74\n\t"
          "v_rcp_f32 v75, v75\n\tv_rcp_f32 v76, v76\n\tv_rcp_f32 v77, v77\n\tv_rcp_f32 v78, v78\n\t"
          "v_rcp_f32 v79, v79\n\tv_rcp_f32 v80, v80\n\tv_rcp_f32 v81, v81\n\tv_rcp_f32 v82, v82\n\t"
          "v_rcp_f32 v83, v83\n\tv_mul_f32 v68, v68, v0\n\tv_mul_f32 v69, v69, v1\n\tv_mul_f32 v70, v70, v2\n\t"
          "v_mul_f32 v71, v71, v3\n\tv_mul_f32 v72, v72, v4\n\tv_mul_f32 v73, v73, v5\n\tv_mul_f32 v74, v74, v6\n\t"
          "v_mul_f32 v75, v75, v7\n\tv_mul_f32 v76, v76, v8\n\tv_mul_f32 v77, v77, v9\n\tv_mul_f32 v78, v78, v10\n\t"
          "v_mul_f32 v79, v79, v11\n\tv_mul_f32 v80, v80, v12\n\tv_mul_f32 v81, v81, v13\n\tv_mul_f32 v82, v82, v14\n\t"
          "v_mul_f32 v83, v83, v15\n\tv_cvt_pk_bf16_f32 v96, v68, v69\n\tv_cvt_pk_bf16_f32 v97, v70, v71\n\tv_cvt_pk_bf16_f32 v98, v72, v73\n\t"
          "v_cvt_pk_bf16_f32 v99, v74, v75\n\tv_cvt_pk_bf16_f32 v104, v76, v77\n\tv_cvt_pk_bf16_f32 v105, v78, v79\n\tv_cvt_pk_bf16_f32 v106, v80, v81\n\t"
          "v_cvt_pk_bf16_f32 v107, v82, v83\n\tbuffer_store_dwordx4 v[96:99], %[l16], %[desc], %[sh] offen offset:0 sc1\n\tbuffer_store_dwordx4 v[104:107], %[l16], %[desc], %[sh] offen offset:2048 sc1\n\tv_fmac_f32 v16, v66, v32\n\t"
          "v_fmac_f32 v17, v66, v33\n\tv_fmac_f32 v18, v66, v34\n\tv_fmac_f32 v19, v66, v35\n\tv_fmac_f32 v20, v66, v36\n\t"
          "v_fmac_f32 v21, v66, v37\n\tv_fmac_f32 v22, v66, v38\n\tv_fmac_f32 v23, v66, v39\n\tv_fmac_f32 v24, v66, v40\n\t"
          "v_fmac_f32 v25, v66, v41\n\tv_fmac_f32 v26, v66, v42\n\tv_fmac_f32 v27, v66, v43\n\tv_fmac_f32 v28, v66, v44\n\t"
          "v_fmac_f32 v29, v66, v45\n\tv_fmac_f32 v30, v66, v46\n\tv_fmac_f32 v31, v66, v47\n\tv_fma_f32 v16, v67, v16, v48\n\t"
          "v_fma_f32 v17, v67, v17, v49\n\tv_fma_f32 v18, v67, v18, v50\n\tv_fma_f32 v19, v67, v19, v51\n\tv_fma_f32 v20, v67, v20, v52\n\t"
          "v_fma_f32 v21, v67, v21, v53\n\tv_fma_f32 v22, v67, v22, v54\n\tv_fma_f32 v23, v67, v23, v55\n\tv_fma_f32 v24, v67, v24, v56\n\t"
          "v_fma_f32 v25, v67, v25, v57\n\tv_fma_f32 v26, v67, v26, v58\n\tv_fma_f32 v27, v67, v27, v59\n\tv_fma_f32 v28, v67, v28, v60\n\t"
          "v_fma_f32 v29, v67, v29, v61\n\tv_fma_f32 v30, v67, v30, v62\n\tv_fma_f32 v31, v67, v31, v63\n\tv_fma_f32 v68, -v16, v16, v84\n\t"
          "v_fma_f32 v69, -v17, v17, v84\n\tv_fma_f32 v70, -v18, v18, v84\n\tv_fma_f32 v71, -v19, v19, v84\n\tv_fma_f32 v72, -v20, v20, v84\n\t"
          "v_fma_f32 v73, -v21, v21, v84\n\tv_fma_f32 v74, -v22, v22, v84\n\tv_fma_f32 v75, -v23, v23, v84\n\tv_fma_f32 v76, -v24, v24, v84\n\t"
          "v_fma_f32 v77, -v25, v25, v84\n\tv_fma_f32 v78, -v26, v26, v84\n\tv_fma_f32 v79, -v27, v27, v84\n\tv_fma_f32 v80, -v28, v28, v84\n\t"
          "v_fma_f32 v81, -v29, v29, v84\n\tv_fma_f32 v82, -v30, v30, v84\n\tv_fma_f32 v83, -v31, v31, v84\n\tv_mul_f32 v68, v68, v16\n\t"
          "v_mul_f32 v69, v69, v17\n\tv_mul_f32 v70, v70, v18\n\tv_mul_f32 v71, v71, v19\n\tv_mul_f32 v72, v72, v20\n\t"
          "v_mul_f32 v73, v73, v21\n\tv_mul_f32 v74, v74, v22\n\tv_mul_f32 v75, v75, v23\n\tv_mul_f32 v76, v76, v24\n\t"
          "v_mul_f32 v77, v77, v25\n\tv_mul_f32 v78, v78, v26\n\tv_mul_f32 v79, v79, v27\n\tv_mul_f32 v80, v80, v28\n\t"
          "v_mul_f32 v81, v81, v29\n\tv_mul_f32 v82, v82, v30\n\tv_mul_f32 v83, v83, v31\n\tv_exp_f32 v68, v68\n\t"
          "v_exp_f32 v69, v69\n\tv_exp_f32 v70, v70\n\tv_exp_f32 v71, v71\n\tv_exp_f32 v72, v72\n\t"
          "v_exp_f32 v73, v73\n\tv_exp_f32 v74, v74\n\tv_exp_f32 v75, v75\n\tv_exp_f32 v76, v76\n\t"
          "v_exp_f32 v77, v77\n\tv_exp_f32 v78, v78\n\tv_exp_f32 v79, v79\n\tv_exp_f32 v80, v80\n\t"
          "v_exp_f32 v81, v81\n\tv_exp_f32 v82, v82\n\tv_exp_f32 v83, v83\n\tv_fma_f32 v68, v68, v85, v85\n\t"
          "v_fma_f32 v69, v69, v85, v85\n\tv_fma_f32 v70, v70, v85, v85\n\tv_fma_f32 v71, v71, v85, v85\n\tv_fma_f32 v72, v72, v85, v85\n\t"
          "v_fma_f32 v73, v73, v85, v85\n\tv_fma_f32 v74, v74, v85, v85\n\tv_fma_f32 v75, v75, v85, v85\n\tv_fma_f32 v76, v76, v85, v85\n\t"
          "v_fma_f32 v77, v77, v85, v85\n\tv_fma_f32 v78, v78, v85, v85\n\tv_fma_f32 v79, v79, v85, v85\n\tv_fma_f32 v80, v80, v85, v85\n\t"
          "v_fma_f32 v81, v81, v85, v85\n\tv_fma_f32 v82, v82, v85, v85\n\tv_fma_f32 v83, v83, v85, v85\n\tv_rcp_f32 v68, v68\n\t"
          "v_rcp_f32 v69, v69\n\tv_rcp_f32 v70, v70\n\tv_rcp_f32 v71, v71\n\tv_rcp_f32 v72, v72\n\t"
          "v_rcp_f32 v73, v73\n\tv_rcp_f32 v74, v74\n\tv_rcp_f32 v75, v75\n\tv_rcp_f32 v76, v76\n\t"
          "v_rcp_f32 v77, v77\n\tv_rcp_f32 v78, v78\n\tv_rcp_f32 v79, v79\n\tv_rcp_f32 v80, v80\n\t"
          "v_rcp_f32 v81, v81\n\tv_rcp_f32 v82, v82\n\tv_rcp_f32 v83, v83\n\tv_mul_f32 v68, v68, v16\n\t"
          "v_mul_f32 v69, v69, v17\n\tv_mul_f32 v70, v70, v18\n\tv_mul_f32 v71, v71, v19\n\tv_mul_f32 v72, v72, v20\n\t"
          "v_mul_f32 v73, v73, v21\n\tv_mul_f32 v74, v74, v22\n\tv_mul_f32 v75, v75, v23\n\tv_mul_f32 v76, v76, v24\n\t"
          "v_mul_f32 v77, v77, v25\n\tv_mul_f32 v78, v78, v26\n\tv_mul_f32 v79, v79, v27\n\tv_mul_f32 v80, v80, v28\n\t"
          "v_mul_f32 v81, v81, v29\n\tv_mul_f32 v82, v82, v30\n\tv_mul_f32 v83, v83, v31\n\tv_cvt_pk_bf16_f32 v100, v68, v69\n\t"
          "v_cvt_pk_bf16_f32 v101, v70, v71\n\tv_cvt_pk_bf16_f32 v102, v72, v73\n\tv_cvt_pk_bf16_f32 v103, v74, v75\n\tv_cvt_pk_bf16_f32 v108, v76, v77\n\t"
          "v_cvt_pk_bf16_f32 v109, v78, v79\n\tv_cvt_pk_bf16_f32 v110, v80, v81\n\tv_cvt_pk_bf16_f32 v111, v82, v83\n\tbuffer_store_dwordx4 v[100:103], %[l16], %[desc], %[sh] offen offset:1024 sc1\n\t"
          "buffer_store_dwordx4 v[108:111], %[l16], %[desc], %[sh] offen offset:3072 sc1\n\ts_branch ME%=\n\tMY%=:\n\ts_setprio 3\n\t"
          "ds_read_b128 v[32:35], %[vst] offset:0\n\tds_read_b128 v[36:39], %[vst] offset:512\n\tds_read_b128 v[40:43], %[vst] offset:1024\n\tds_read_b128 v[44:47], %[vst] offset:1536\n\t"
          "ds_read_b128 v[48:51], %[vst] offset:2048\n\tds_read_b128 v[52:55], %[vst] offset:2560\n\tds_read_b128 v[56:59], %[vst] offset:3072\n\tds_read_b128 v[60:63], %[vst] offset:3584\n\t"
          "s_waitcnt lgkmcnt(6)\n\tv_add_f32 v32, v32, v36\n\tv_add_f32 v33, v33, v37\n\tv_add_f32 v34, v34, v38\n\t"
          "v_add_f32 v35, v35, v39\n\ts_waitcnt lgkmcnt(4)\n\tv_add_f32 v40, v40, v44\n\tv_add_f32 v41, v41, v45\n\t"
          "v_add_f32 v42, v42, v46\n\tv_add_f32 v43, v43, v47\n\ts_waitcnt lgkmcnt(2)\n\tv_add_f32 v48, v48, v52\n\t"
          "v_add_f32 v49, v49, v53\n\tv_add_f32 v50, v50, v54\n\tv_add_f32 v51, v51, v55\n\ts_waitcnt lgkmcnt(0)\n\t"
          "v_add_f32 v56, v56, v60\n\tv_add_f32 v57, v57, v61\n\tv_add_f32 v58, v58, v62\n\tv_add_f32 v59, v59, v63\n\t"
          "v_add_f32 v32, v32, v40\n\tv_add_f32 v33, v33, v41\n\tv_add_f32 v34, v34, v42\n\tv_add_f32 v35, v35, v43\n\t"
          "v_add_f32 v48, v48, v56\n\tv_add_f32 v49, v49, v57\n\tv_add_f32 v50, v50, v58\n\tv_add_f32 v51, v51, v59\n\t"
          "v_add_f32 v32, v32, v48\n\tv_add_f32 v33, v33, v49\n\tv_add_f32 v34, v34, v50\n\tv_add_f32 v35, v35, v51\n\t"
          "v_mul_f32 v64, 0xbb000000, v32\n\tv_mul_f32 v66, 0xbb000000, v34\n\tv_mul_f32 v68, 0x3b000000, v33\n\tv_mul_f32 v69, 0x3b000000, v35\n\t"
          "v_fma_f32 v68, -v64, v64, v68\n\tv_fma_f32 v69, -v66, v66, v69\n\tv_max_f32 v68, 0, v68\n\tv_max_f32 v69, 0, v69\n\t"
          "v_add_f32 v70, 0x3727c5ac, v68\n\tv_add_f32 v71, 0x3727c5ac, v69\n\tv_rsq_f32 v65, v70\n\tv_rsq_f32 v67, v71\n\t"
          "s_nop 0\n\tv_mul_f32 v68, v68, v65\n\tv_mul_f32 v69, v69, v67\n\tv_fmaak_f32 v68, v68, v65, 0x3727c5ac\n\t"
          "v_fmaak_f32 v69, v69, v67, 0x3727c5ac\n\tv_rsq_f32 v68, v68\n\tv_rsq_f32 v69, v69\n\tv_mul_f32 v65, 0x3eeff558, v65\n\t"
          "v_mul_f32 v67, 0x3eeff558, v67\n\tv_mul_f32 v65, v65, v68\n\tv_mul_f32 v67, v67, v69\n\tv_mov_b32 v84, 0xc09d30fc\n\t"
          "v_mov_b32 v85, 0x3eeff558\n\tds_read_b128 v[32:35], %[vtab] offset:128\n\tds_read_b128 v[36:39], %[vtab] offset:160\n\tds_read_b128 v[40:43], %[vtab] offset:192\n\t"
          "ds_read_b128 v[44:47], %[vtab] offset:224\n\tds_read_b128 v[48:51], %[vtab] offset:256\n\tds_read_b128 v[52:55], %[vtab] offset:288\n\tds_read_b128 v[56:59], %[vtab] offset:320\n\t"
          "ds_read_b128 v[60:63], %[vtab] offset:352\n\ts_cmp_lg_u32 %[w], 7\n\ts_cbranch_scc1 MB%=\n\tv_mov_b32 v64, v66\n\t"
          "v_mov_b32 v65, v67\n\tMB%=:\n\ts_waitcnt lgkmcnt(0)\n\ts_setprio 1\n\t"
          "s_cmp_ge_u32 %[w], 6\n\ts_cbranch_scc1 MZ%=\n\tds_read_b128 v[96:99], %[vr] offset:4096\n\tds_read_b128 v[100:103], %[vr] offset:36864\n\t"
          "ds_read_b128 v[104:107], %[vr] offset:5120\n\tds_read_b128 v[108:111], %[vr] offset:37888\n\tds_read_b128 v[112:115], %[vr] offset:6144\n\tds_read_b128 v[116:119], %[vr] offset:38912\n\t"
          "ds_read_b128 v[120:123], %[vr] offset:7168\n\tds_read_b128 v[124:127], %[vr] offset:39936\n\ts_waitcnt lgkmcnt(7)\n\tv_mfma_f32_32x32x16_bf16 v[0:15], v[144:147], v[96:99], v[0:15]\n\t"
          "ds_read_b128 v[96:99], %[vr] offset:8192\n\ts_waitcnt lgkmcnt(7)\n\tv_mfma_f32_32x32x16_bf16 v[16:31], v[144:147], v[100:103], v[16:31]\n\tds_read_b128 v[100:103], %[vr] offset:40960\n\t"
          "s_waitcnt lgkmcnt(7)\n\tv_mfma_f32_32x32x16_bf16 v[0:15], v[148:151], v[104:107], v[0:15]\n\tds_read_b128 v[104:107], %[vr] offset:9216\n\ts_waitcnt lgkmcnt(7)\n\t"
          "v_mfma_f32_32x32x16_bf16 v[16:31], v[148:151], v[108:111], v[16:31]\n\tds_read_b128 v[108:111], %[vr] offset:41984\n\ts_waitcnt lgkmcnt(7)\n\tv_mfma_f32_32x32x16_bf16 v[0:15], v[152:155], v[112:115], v[0:15]\n\t"
          "ds_read_b128 v[112:115], %[vr] offset:10240\n\ts_waitcnt lgkmcnt(7)\n\tv_mfma_f32_32x32x16_bf16 v[16:31], v[152:155], v[116:119], v[16:31]\n\tds_read_b128 v[116:119], %[vr] offset:43008\n\t"
          "s_waitcnt lgkmcnt(7)\n\tv_mfma_f32_32x32x16_bf16 v[0:15], v[156:159], v[120:123], v[0:15]\n\tds_read_b128 v[120:123], %[vr] offset:11264\n\ts_waitcnt lgkmcnt(7)\n\t"
          "v_mfma_f32_32x32x16_bf16 v[16:31], v[156:159], v[124:127], v[16:31]\n\tds_read_b128 v[124:127], %[vr] offset:44032\n\tbuffer_load_dwordx4 v[144:147], %[l16], %[desc], %[sw] offen offset:0 sc1\n\ts_waitcnt lgkmcnt(7)\n\t"
          "v_mfma_f32_32x32x16_bf16 v[0:15], v[160:163], v[96:99], v[0:15]\n\tds_read_b128 v[96:99], %[vr] offset:12288\n\ts_waitcnt lgkmcnt(7)\n\tv_mfma_f32_32x32x16_bf16 v[16:31], v[160:163], v[100:103], v[16:31]\n\t"
          "ds_read_b128 v[100:103], %[vr] offset:45056\n\tbuffer_load_dwordx4 v[148:151], %[l16], %[desc], %[sw] offen offset:1024 sc1\n\ts_waitcnt lgkmcnt(7)\n\tv_mfma_f32_32x32x16_bf16 v[0:15], v[164:167], v[104:107], v[0:15]\n\t"
          "ds_read_b128 v[104:107], %[vr] offset:13312\n\ts_waitcnt lgkmcnt(7)\n\tv_mfma_f32_32x32x16_bf16 v[16:31], v[164:167], v[108:111], v[16:31]\n\tds_read_b128 v[108:111], %[vr] offset:46080\n\t"
          "buffer_load_dwordx4 v[152:155], %[l16], %[desc], %[sw] offen offset:2048 sc1\n\ts_waitcnt lgkmcnt(7)\n\tv_mfma_f32_32x32x16_bf16 v[0:15], v[168:171], v[112:115], v[0:15]\n\tds_read_b128 v[112:115], %[vr] offset:14336\n\t"
          "s_waitcnt lgkmcnt(7)\n\tv_mfma_f32_32x32x16_bf16 v[16:31], v[168:171], v[116:119], v[16:31]\n\tds_read_b128 v[116:119], %[vr] offset:47104\n\tbuffer_load_dwordx4 v[156:159], %[l16], %[desc], %[sw] offen offset:3072 sc1\n\t"
          "s_add_u32 %[sw], %[sw], 4096\n\ts_waitcnt lgkmcnt(7)\n\tv_mfma_f32_32x32x16_bf16 v[0:15], v[172:175], v[120:123], v[0:15]\n\tds_read_b128 v[120:123], %[vr] offset:15360\n\t"
          "s_waitcnt lgkmcnt(7)\n\tv_mfma_f32_32x32x16_bf16 v[16:31], v[172:175], v[124:127], v[16:31]\n\tds_read_b128 v[124:127], %[vr] offset:48128\n\tbuffer_load_dwordx4 v[160:163], %[l16], %[desc], %[sw] offen offset:0 sc1\n\t"
          "s_waitcnt lgkmcnt(7)\n\tv_mfma_f32_32x32x16_bf16 v[0:15], v[176:179], v[96:99], v[0:15]\n\tds_read_b128 v[96:99], %[vr] offset:16384\n\ts_waitcnt lgkmcnt(7)\n\t"
          "v_mfma_f32_32x32x16_bf16 v[16:31], v[176:179], v[100:103], v[16:31]\n\tds_read_b128 v[100:103], %[vr] offset:49152\n\tbuffer_load_dwordx4 v[164:167], %[l16], %[desc], %[sw] offen offset:1024 sc1\n\ts_waitcnt lgkmcnt(7)\n\t"
          "v_mfma_f32_32x32x16_bf16 v[0:15], v[180:183], v[104:107], v[0:15]\n\tds_read_b128 v[104:107], %[vr] offset:17408\n\ts_waitcnt lgkmcnt(7)\n\tv_mfma_f32_32x32x16_bf16 v[16:31], v[180:183], v[108:111], v[16:31]\n\t"
          "ds_read_b128 v[108:111], %[vr] offset:50176\n\tbuffer_load_dwordx4 v[168:171], %[l16], %[desc], %[sw] offen offset:2048 sc1\n\ts_waitcnt lgkmcnt(7)\n\tv_mfma_f32_32x32x16_bf16 v[0:15], v[184:187], v[112:115], v[0:15]\n\t"
          "ds_read_b128 v[112:115], %[vr] offset:18432\n\ts_waitcnt lgkmcnt(7)\n\tv_mfma_f32_32x32x16_bf16 v[16:31], v[184:187], v[116:119], v[16:31]\n\tds_read_b128 v[116:119], %[vr] offset:51200\n\t"
          "buffer_load_dwordx4 v[172:175], %[l16], %[desc], %[sw] offen offset:3072 sc1\n\ts_add_u32 %[sw], %[sw], 4096\n\tbuffer_load_dwordx4 v[176:179], %[l16], %[desc], %[sw] offen offset:0 sc1\n\ts_waitcnt lgkmcnt(7)\n\t"
          "v_mfma_f32_32x32x16_bf16 v[0:15], v[188:191], v[120:123], v[0:15]\n\tds_read_b128 v[120:123], %[vr] offset:19456\n\ts_waitcnt lgkmcnt(7)\n\tv_mfma_f32_32x32x16_bf16 v[16:31], v[188:191], v[124:127], v[16:31]\n\t"
          "ds_read_b128 v[124:127], %[vr] offset:52224\n\tbuffer_load_dwordx4 v[180:183], %[l16], %[desc], %[sw] offen offset:1024 sc1\n\ts_waitcnt lgkmcnt(7)\n\tv_mfma_f32_32x32x16_bf16 v[0:15], v[192:195], v[96:99], v[0:15]\n\t"
          "ds_read_b128 v[96:99], %[vr] offset:20480\n\ts_waitcnt lgkmcnt(7)\n\tv_mfma_f32_32x32x16_bf16 v[16:31], v[192:195], v[100:103], v[16:31]\n\tds_read_b128 v[100:103], %[vr] offset:53248\n\t"
          "buffer_load_dwordx4 v[184:187], %[l16], %[desc], %[sw] offen offset:2048 sc1\n\ts_waitcnt lgkmcnt(7)\n\tv_mfma_f32_32x32x16_bf16 v[0:15], v[196:199], v[104:107], v[0:15]\n\tds_read_b128 v[104:107], %[vr] offset:21504\n\t"
          "s_waitcnt lgkmcnt(7)\n\tv_mfma_f32_32x32x16_bf16 v[16:31], v[196:199], v[108:111], v[16:31]\n\tds_read_b128 v[108:111], %[vr] offset:54272\n\tbuffer_load_dwordx4 v[188:191], %[l16], %[desc], %[sw] offen offset:3072 sc1\n\t"
          "s_add_u32 %[sw], %[sw], 4096\n\ts_waitcnt lgkmcnt(7)\n\tv_mfma_f32_32x32x16_bf16 v[0:15], v[200:203], v[112:115], v[0:15]\n\tds_read_b128 v[112:115], %[vr] offset:22528\n\t"
          "s_waitcnt lgkmcnt(7)\n\tv_mfma_f32_32x32x16_bf16 v[16:31], v[200:203], v[116:119], v[16:31]\n\tds_read_b128 v[116:119], %[vr] offset:55296\n\tbuffer_load_dwordx4 v[192:195], %[l16], %[desc], %[sw] offen offset:0 sc1\n\t"
          "s_waitcnt lgkmcnt(7)\n\tv_mfma_f32_32x32x16_bf16 v[0:15], v[204:207], v[120:123], v[0:15]\n\tds_read_b128 v[120:123], %[vr] offset:23552\n\ts_waitcnt lgkmcnt(7)\n\t"
          "v_mfma_f32_32x32x16_bf16 v[16:31], v[204:207], v[124:127], v[16:31]\n\tds_read_b128 v[124:127], %[vr] offset:56320\n\tbuffer_load_dwordx4 v[196:199], %[l16], %[desc], %[sw] offen offset:1024 sc1\n\ts_waitcnt lgkmcnt(7)\n\t"
          "v_mfma_f32_32x32x16_bf16 v[0:15], v[208:211], v[96:99], v[0:15]\n\tds_read_b128 v[96:99], %[vr] offset:24576\n\ts_waitcnt lgkmcnt(7)\n\tv_mfma_f32_32x32x16_bf16 v[16:31], v[208:211], v[100:103], v[16:31]\n\t"
          "ds_read_b128 v[100:103], %[vr] offset:57344\n\tbuffer_load_dwordx4 v[200:203], %[l16], %[desc], %[sw] offen offset:2048 sc1\n\ts_waitcnt lgkmcnt(7)\n\tv_mfma_f32_32x32x16_bf16 v[0:15], v[212:215], v[104:107], v[0:15]\n\t"
          "ds_read_b128 v[104:107], %[vr] offset:25600\n\ts_waitcnt lgkmcnt(7)\n\tv_mfma_f32_32x32x16_bf16 v[16:31], v[212:215], v[108:111], v[16:31]\n\tds_read_b128 v[108:111], %[vr] offset:58368\n\t"
          "buffer_load_dwordx4 v[204:207], %[l16], %[desc], %[sw] offen offset:3072 sc1\n\ts_add_u32 %[sw], %[sw], 4096\n\tbuffer_load_dwordx4 v[208:211], %[l16], %[desc], %[sw] offen offset:0 sc1\n\ts_waitcnt lgkmcnt(7)\n\t"
          "v_mfma_f32_32x32x16_bf16 v[0:15], v[216:219], v[112:115], v[0:15]\n\tds_read_b128 v[112:115], %[vr] offset:26624\n\ts_waitcnt lgkmcnt(7)\n\tv_mfma_f32_32x32x16_bf16 v[16:31], v[216:219], v[116:119], v[16:31]\n\t"
          "ds_read_b128 v[116:119], %[vr] offset:59392\n\tbuffer_load_dwordx4 v[212:215], %[l16], %[desc], %[sw] offen offset:1024 sc1\n\ts_waitcnt lgkmcnt(7)\n\tv_mfma_f32_32x32x16_bf16 v[0:15], v[220:223], v[120:123], v[0:15]\n\t"
          "ds_read_b128 v[120:123], %[vr] offset:27648\n\ts_waitcnt lgkmcnt(7)\n\tv_mfma_f32_32x32x16_bf16 v[16:31], v[220:223], v[124:127], v[16:31]\n\tds_read_b128 v[124:127], %[vr] offset:60416\n\t"
          "s_waitcnt lgkmcnt(7)\n\tv_mfma_f32_32x32x16_bf16 v[0:15], v[224:227], v[96:99], v[0:15]\n\tds_read_b128 v[96:99], %[vr] offset:28672\n\ts_waitcnt lgkmcnt(7)\n\t"
          "v_mfma_f32_32x32x16_bf16 v[16:31], v[224:227], v[100:103], v[16:31]\n\tds_read_b128 v[100:103], %[vr] offset:61440\n\ts_waitcnt lgkmcnt(7)\n\tv_mfma_f32_32x32x16_bf16 v[0:15], v[228:231], v[104:107], v[0:15]\n\t"
          "ds_read_b128 v[104:107], %[vr] offset:29696\n\ts_waitcnt lgkmcnt(7)\n\tv_mfma_f32_32x32x16_bf16 v[16:31], v[228:231], v[108:111], v[16:31]\n\tds_read_b128 v[108:111], %[vr] offset:62464\n\t"
          "s_waitcnt lgkmcnt(7)\n\tv_mfma_f32_32x32x16_bf16 v[0:15], v[232:235], v[112:115], v[0:15]\n\tds_read_b128 v[112:115], %[vr] offset:30720\n\ts_waitcnt lgkmcnt(7)\n\t"
          "v_mfma_f32_32x32x16_bf16 v[16:31], v[232:235], v[116:119], v[16:31]\n\tds_read_b128 v[116:119], %[vr] offset:63488\n\ts_waitcnt lgkmcnt(7)\n\tv_mfma_f32_32x32x16_bf16 v[0:15], v[236:239], v[120:123], v[0:15]\n\t"
          "ds_read_b128 v[120:123], %[vr] offset:31744\n\ts_waitcnt lgkmcnt(7)\n\tv_mfma_f32_32x32x16_bf16 v[16:31], v[236:239], v[124:127], v[16:31]\n\tds_read_b128 v[124:127], %[vr] offset:64512\n\t"
          "s_waitcnt lgkmcnt(7)\n\tv_mfma_f32_32x32x16_bf16 v[0:15], v[240:243], v[96:99], v[0:15]\n\ts_waitcnt lgkmcnt(6)\n\tv_mfma_f32_32x32x16_bf16 v[16:31], v[240:243], v[100:103], v[16:31]\n\t"
          "s_waitcnt lgkmcnt(5)\n\tv_mfma_f32_32x32x16_bf16 v[0:15], v[244:247], v[104:107], v[0:15]\n\ts_waitcnt lgkmcnt(4)\n\tv_mfma_f32_32x32x16_bf16 v[16:31], v[244:247], v[108:111], v[16:31]\n\t"
          "s_waitcnt lgkmcnt(3)\n\tv_mfma_f32_32x32x16_bf16 v[0:15], v[248:251], v[112:115], v[0:15]\n\ts_waitcnt lgkmcnt(2)\n\tv_mfma_f32_32x32x16_bf16 v[16:31], v[248:251], v[116:119], v[16:31]\n\t"
          "s_waitcnt lgkmcnt(1)\n\tv_mfma_f32_32x32x16_bf16 v[0:15], v[252:255], v[120:123], v[0:15]\n\ts_waitcnt lgkmcnt(0)\n\tv_mfma_f32_32x32x16_bf16 v[16:31], v[252:255], v[124:127], v[16:31]\n\t"
          "s_setprio 3\n\tv_mov_b32 v112, %[mef]\n\tv_mov_b32 v113, %[lay1]\n\ts_nop 9\n\t"
          "ds_write_b128 %[vex], v[16:19] offset:0\n\tds_write_b128 %[vex], v[20:23] offset:1024\n\tds_write_b128 %[vex], v[24:27] offset:2048\n\tds_write_b128 %[vex], v[28:31] offset:3072\n\t"
          "ds_write_b32 v112, v113\n\tv_pk_fma_f32 v[0:1], v[64:65], v[32:33], v[0:1] op_sel_hi:[0,1,1]\n\tv_pk_fma_f32 v[2:3], v[64:65], v[34:35], v[2:3] op_sel_hi:[0,1,1]\n\tv_pk_fma_f32 v[4:5], v[64:65], v[36:37], v[4:5] op_sel_hi:[0,1,1]\n\t"
          "v_pk_fma_f32 v[6:7], v[64:65], v[38:39], v[6:7] op_sel_hi:[0,1,1]\n\tv_pk_fma_f32 v[8:9], v[64:65], v[40:41], v[8:9] op_sel_hi:[0,1,1]\n\tv_pk_fma_f32 v[10:11], v[64:65], v[42:43], v[10:11] op_sel_hi:[0,1,1]\n\tv_pk_fma_f32 v[12:13], v[64:65], v[44:45], v[12:13] op_sel_hi:[0,1,1]\n\t"
          "v_pk_fma_f32 v[14:15], v[64:65], v[46:47], v[14:15] op_sel_hi:[0,1,1]\n\tv_pk_fma_f32 v[0:1], v[64:65], v[0:1], v[48:49] op_sel:[1,0,0]\n\tv_pk_fma_f32 v[2:3], v[64:65], v[2:3], v[50:51] op_sel:[1,0,0]\n\tv_pk_fma_f32 v[4:5], v[64:65], v[4:5], v[52:53] op_sel:[1,0,0]\n\t"
          "v_pk_fma_f32 v[6:7], v[64:65], v[6:7], v[54:55] op_sel:[1,0,0]\n\tv_pk_fma_f32 v[8:9], v[64:65], v[8:9], v[56:57] op_sel:[1,0,0]\n\tv_pk_fma_f32 v[10:11], v[64:65], v[10:11], v[58:59] op_sel:[1,0,0]\n\tv_pk_fma_f32 v[12:13], v[64:65], v[12:13], v[60:61] op_sel:[1,0,0]\n\t"
          "v_pk_fma_f32 v[14:15], v[64:65], v[14:15], v[62:63] op_sel:[1,0,0]\n\tv_pk_fma_f32 v[68:69], v[0:1], v[0:1], v[84:85] op_sel_hi:[1,1,0] neg_lo:[1,0,0] neg_hi:[1,0,0]\n\tv_pk_fma_f32 v[70:71], v[2:3], v[2:3], v[84:85] op_sel_hi:[1,1,0] neg_lo:[1,0,0] neg_hi:[1,0,0]\n\tv_pk_fma_f32 v[72:73], v[4:5], v[4:5], v[84:85] op_sel_hi:[1,1,0] neg_lo:[1,0,0] neg_hi:[1,0,0]\n\t"
          "v_pk_fma_f32 v[74:75], v[6:7], v[6:7], v[84:85] op_sel_hi:[1,1,0] neg_lo:[1,0,0] neg_hi:[1,0,0]\n\tv_pk_fma_f32 v[76:77], v[8:9], v[8:9], v[84:85] op_sel_hi:[1,1,0] neg_lo:[1,0,0] neg_hi:[1,0,0]\n\tv_pk_fma_f32 v[78:79], v[10:11], v[10:11], v[84:85] op_sel_hi:[1,1,0] neg_lo:[1,0,0] neg_hi:[1,0,0]\n\tv_pk_fma_f32 v[80:81], v[12:13], v[12:13], v[84:85] op_sel_hi:[1,1,0] neg_lo:[1,0,0] neg_hi:[1,0,0]\n\t"
          "v_pk_fma_f32 v[82:83], v[14:15], v[14:15], v[84:85] op_sel_hi:[1,1,0] neg_lo:[1,0,0] neg_hi:[1,0,0]\n\tv_pk_mul_f32 v[68:69], v[68:69], v[0:1]\n\tv_pk_mul_f32 v[70:71], v[70:71], v[2:3]\n\tv_pk_mul_f32 v[72:73], v[72:73], v[4:5]\n\t"
          "v_pk_mul_f32 v[74:75], v[74:75], v[6:7]\n\tv_pk_mul_f32 v[76:77], v[76:77], v[8:9]\n\tv_pk_mul_f32 v[78:79], v[78:79], v[10:11]\n\tv_pk_mul_f32 v[80:81], v[80:81], v[12:13]\n\t"
          "v_pk_mul_f32 v[82:83], v[82:83], v[14:15]\n\tv_exp_f32 v68, v68\n\tv_exp_f32 v69, v69\n\tv_exp_f32 v70, v70\n\t"
          "v_exp_f32 v71, v71\n\tv_exp_f32 v72, v72\n\tv_exp_f32 v73, v73\n\tv_exp_f32 v74, v74\n\t"
          "v_exp_f32 v75, v75\n\tv_exp_f32 v76, v76\n\tv_exp_f32 v77, v77\n\tv_exp_f32 v78, v78\n\t"
          "v_exp_f32 v79, v79\n\tv_exp_f32 v80, v80\n\tv_exp_f32 v81, v81\n\tv_exp_f32 v82, v82\n\t"
          "v_exp_f32 v83, v83\n\tv_pk_fma_f32 v[68:69], v[68:69], v[84:85], v[84:85] op_sel:[0,1,1]\n\tv_pk_fma_f32 v[70:71], v[70:71], v[84:85], v[84:85] op_sel:[0,1,1]\n\tv_pk_fma_f32 v[72:73], v[72:73], v[84:85], v[84:85] op_sel:[0,1,1]\n\t"
          "v_pk_fma_f32 v[74:75], v[74:75], v[84:85], v[84:85] op_sel:[0,1,1]\n\tv_pk_fma_f32 v[76:77], v[76:77], v[84:85], v[84:85] op_sel:[0,1,1]\n\tv_pk_fma_f32 v[78:79], v[78:79], v[84:85], v[84:85] op_sel:[0,1,1]\n\tv_pk_fma_f32 v[80:81], v[80:81], v[84:85], v[84:85] op_sel:[0,1,1]\n\t"
          "v_pk_fma_f32 v[82:83], v[82:83], v[84:85], v[84:85] op_sel:[0,1,1]\n\tv_rcp_f32 v68, v68\n\tv_rcp_f32 v69, v69\n\tv_rcp_f32 v70, v70\n\t"
          "v_rcp_f32 v71, v71\n\tv_rcp_f32 v72, v72\n\tv_rcp_f32 v73, v73\n\tv_rcp_f32 v74, v74\n\t"
          "v_rcp_f32 v75, v75\n\tv_rcp_f32 v76, v76\n\tv_rcp_f32 v77, v77\n\tv_rcp_f32 v78, v78\n\t"
          "v_rcp_f32 v79, v79\n\tv_rcp_f32 v80, v80\n\tv_rcp_f32 v81, v81\n\tv_rcp_f32 v82, v82\n\t"
          "v_rcp_f32 v83, v83\n\tv_pk_mul_f32 v[68:69], v[68:69], v[0:1]\n\tv_pk_mul_f32 v[70:71], v[70:71], v[2:3]\n\tv_pk_mul_f32 v[72:73], v[72:73], v[4:5]\n\t"
          "v_pk_mul_f32 v[74:75], v[74:75], v[6:7]\n\tv_pk_mul_f32 v[76:77], v[76:77], v[8:9]\n\tv_pk_mul_f32 v[78:79], v[78:79], v[10:11]\n\tv_pk_mul_f32 v[80:81], v[80:81], v[12:13]\n\t"
          "v_pk_mul_f32 v[82:83], v[82:83], v[14:15]\n\tv_cvt_pk_bf16_f32 v96, v68, v69\n\tv_cvt_pk_bf16_f32 v97, v70, v71\n\tv_cvt_pk_bf16_f32 v98, v72, v73\n\t"
          "v_cvt_pk_bf16_f32 v99, v74, v75\n\tv_cvt_pk_bf16_f32 v104, v76, v77\n\tv_cvt_pk_bf16_f32 v105, v78, v79\n\tv_cvt_pk_bf16_f32 v106, v80, v81\n\t"
          "v_cvt_pk_bf16_f32 v107, v82, v83\n\tbuffer_store_dwordx4 v[96:99], %[l16], %[desc], %[sh] offen offset:0 sc1\n\tbuffer_store_dwordx4 v[104:107], %[l16], %[desc], %[sh] offen offset:2048 sc1\n\ts_branch ME%=\n\t"
          "MZ%=:\n\tds_read_b128 v[96:99], %[vr] offset:4096\n\tds_read_b128 v[100:103], %[vr] offset:5120\n\tds_read_b128 v[104:107], %[vr] offset:6144\n\t"
          "ds_read_b128 v[108:111], %[vr] offset:7168\n\tds_read_b128 v[112:115], %[vr] offset:8192\n\tds_read_b128 v[116:119], %[vr] offset:9216\n\tds_read_b128 v[120:123], %[vr] offset:10240\n\t"
          "ds_read_b128 v[124:127], %[vr] offset:11264\n\ts_waitcnt lgkmcnt(7)\n\tv_mfma_f32_32x32x16_bf16 v[0:15], v[144:147], v[96:99], v[0:15]\n\tds_read_b128 v[96:99], %[vr] offset:12288\n\t"
          "s_waitcnt lgkmcnt(7)\n\tv_mfma_f32_32x32x16_bf16 v[0:15], v[148:151], v[100:103], v[0:15]\n\tds_read_b128 v[100:103], %[vr] offset:13312\n\ts_waitcnt lgkmcnt(7)\n\t"
          "v_mfma_f32_32x32x16_bf16 v[0:15], v[152:155], v[104:107], v[0:15]\n\tds_read_b128 v[104:107], %[vr] offset:14336\n\ts_waitcnt lgkmcnt(7)\n\tv_mfma_f32_32x32x16_bf16 v[0:15], v[156:159], v[108:111], v[0:15]\n\t"
          "ds_read_b128 v[108:111], %[vr] offset:15360\n\tbuffer_load_dwordx4 v[144:147], %[l16], %[desc], %[sw] offen offset:0 sc1\n\ts_waitcnt lgkmcnt(7)\n\tv_mfma_f32_32x32x16_bf16 v[0:15], v[160:163], v[112:115], v[0:15]\n\t"
          "ds_read_b128 v[112:115], %[vr] offset:16384\n\tbuffer_load_dwordx4 v[148:151], %[l16], %[desc], %[sw] offen offset:1024 sc1\n\ts_waitcnt lgkmcnt(7)\n\tv_mfma_f32_32x32x16_bf16 v[0:15], v[164:167], v[116:119], v[0:15]\n\t"
          "ds_read_b128 v[116:119], %[vr] offset:17408\n\tbuffer_load_dwordx4 v[152:155], %[l16], %[desc], %[sw] offen offset:2048 sc1\n\ts_waitcnt lgkmcnt(7)\n\tv_mfma_f32_32x32x16_bf16 v[0:15], v[168:171], v[120:123], v[0:15]\n\t"
          "ds_read_b128 v[120:123], %[vr] offset:18432\n\tbuffer_load_dwordx4 v[156:159], %[l16], %[desc], %[sw] offen offset:3072 sc1\n\ts_add_u32 %[sw], %[sw], 4096\n\ts_waitcnt lgkmcnt(7)\n\t"
          "v_mfma_f32_32x32x16_bf16 v[0:15], v[172:175], v[124:127], v[0:15]\n\tds_read_b128 v[124:127], %[vr] offset:19456\n\tbuffer_load_dwordx4 v[160:163], %[l16], %[desc], %[sw] offen offset:0 sc1\n\ts_waitcnt lgkmcnt(7)\n\t"
          "v_mfma_f32_32x32x16_bf16 v[0:15], v[176:179], v[96:99], v[0:15]\n\tds_read_b128 v[96:99], %[vr] offset:20480\n\tbuffer_load_dwordx4 v[164:167], %[l16], %[desc], %[sw] offen offset:1024 sc1\n\ts_waitcnt lgkmcnt(7)\n\t"
          "v_mfma_f32_32x32x16_bf16 v[0:15], v[180:183], v[100:103], v[0:15]\n\tds_read_b128 v[100:103], %[vr] offset:21504\n\tbuffer_load_dwordx4 v[168:171], %[l16], %[desc], %[sw] offen offset:2048 sc1\n\ts_waitcnt lgkmcnt(7)\n\t"
          "v_mfma_f32_32x32x16_bf16 v[0:15], v[184:187], v[104:107], v[0:15]\n\tds_read_b128 v[104:107], %[vr] offset:22528\n\tbuffer_load_dwordx4 v[172:175], %[l16], %[desc], %[sw] offen offset:3072 sc1\n\ts_add_u32 %[sw], %[sw], 4096\n\t"
          "buffer_load_dwordx4 v[176:179], %[l16], %[desc], %[sw] offen offset:0 sc1\n\ts_waitcnt lgkmcnt(7)\n\tv_mfma_f32_32x32x16_bf16 v[0:15], v[188:191], v[108:111], v[0:15]\n\tds_read_b128 v[108:111], %[vr] offset:23552\n\t"
          "buffer_load_dwordx4 v[180:183], %[l16], %[desc], %[sw] offen offset:1024 sc1\n\ts_waitcnt lgkmcnt(7)\n\tv_mfma_f32_32x32x16_bf16 v[0:15], v[192:195], v[112:115], v[0:15]\n\tds_read_b128 v[112:115], %[vr] offset:24576\n\t"
          "buffer_load_dwordx4 v[184:187], %[l16], %[desc], %[sw] offen offset:2048 sc1\n\ts_waitcnt lgkmcnt(7)\n\tv_mfma_f32_32x32x16_bf16 v[0:15], v[196:199], v[116:119], v[0:15]\n\tds_read_b128 v[116:119], %[vr] offset:25600\n\t"
          "buffer_load_dwordx4 v[188:191], %[l16], %[desc], %[sw] offen offset:3072 sc1\n\ts_add_u32 %[sw], %[sw], 4096\n\ts_waitcnt lgkmcnt(7)\n\tv_mfma_f32_32x32x16_bf16 v[0:15], v[200:203], v[120:123], v[0:15]\n\t"
          "ds_read_b128 v[120:123], %[vr] offset:26624\n\tbuffer_load_dwordx4 v[192:195], %[l16], %[desc], %[sw] offen offset:0 sc1\n\ts_waitcnt lgkmcnt(7)\n\tv_mfma_f32_32x32x16_bf16 v[0:15], v[204:207], v[124:127], v[0:15]\n\t"
          "ds_read_b128 v[124:127], %[vr] offset:27648\n\tbuffer_load_dwordx4 v[196:199], %[l16], %[desc], %[sw] offen offset:1024 sc1\n\ts_waitcnt lgkmcnt(7)\n\tv_mfma_f32_32x32x16_bf16 v[0:15], v[208:211], v[96:99], v[0:15]\n\t"
          "ds_read_b128 v[96:99], %[vr] offset:28672\n\tbuffer_load_dwordx4 v[200:203], %[l16], %[desc], %[sw] offen offset:2048 sc1\n\ts_waitcnt lgkmcnt(7)\n\tv_mfma_f32_32x32x16_bf16 v[0:15], v[212:215], v[100:103], v[0:15]\n\t"
          "ds_read_b128 v[100:103], %[vr] offset:29696\n\tbuffer_load_dwordx4 v[204:207], %[l16], %[desc], %[sw] offen offset:3072 sc1\n\ts_add_u32 %[sw], %[sw], 4096\n\tbuffer_load_dwordx4 v[208:211], %[l16], %[desc], %[sw] offen offset:0 sc1\n\t"
          "s_waitcnt lgkmcnt(7)\n\tv_mfma_f32_32x32x16_bf16 v[0:15], v[216:219], v[104:107], v[0:15]\n\tds_read_b128 v[104:107], %[vr] offset:30720\n\tbuffer_load_dwordx4 v[212:215], %[l16], %[desc], %[sw] offen offset:1024 sc1\n\t"
          "s_waitcnt lgkmcnt(7)\n\tv_mfma_f32_32x32x16_bf16 v[0:15], v[220:223], v[108:111], v[0:15]\n\tds_read_b128 v[108:111], %[vr] offset:31744\n\ts_waitcnt lgkmcnt(7)\n\t"
          "v_mfma_f32_32x32x16_bf16 v[0:15], v[224:227], v[112:115], v[0:15]\n\ts_waitcnt lgkmcnt(6)\n\tv_mfma_f32_32x32x16_bf16 v[0:15], v[228:231], v[116:119], v[0:15]\n\ts_waitcnt lgkmcnt(5)\n\t"
          "v_mfma_f32_32x32x16_bf16 v[0:15], v[232:235], v[120:123], v[0:15]\n\ts_waitcnt lgkmcnt(4)\n\tv_mfma_f32_32x32x16_bf16 v[0:15], v[236:239], v[124:127], v[0:15]\n\ts_waitcnt lgkmcnt(3)\n\t"
          "v_mfma_f32_32x32x16_bf16 v[0:15], v[240:243], v[96:99], v[0:15]\n\ts_waitcnt lgkmcnt(2)\n\tv_mfma_f32_32x32x16_bf16 v[0:15], v[244:247], v[100:103], v[0:15]\n\ts_waitcnt lgkmcnt(1)\n\t"
          "v_mfma_f32_32x32x16_bf16 v[0:15], v[248:251], v[104:107], v[0:15]\n\ts_waitcnt lgkmcnt(0)\n\tv_mfma_f32_32x32x16_bf16 v[0:15], v[252:255], v[108:111], v[0:15]\n\ts_setprio 3\n\t"
          "s_nop 12\n\tv_pk_fma_f32 v[0:1], v[64:65], v[32:33], v[0:1] op_sel_hi:[0,1,1]\n\tv_pk_fma_f32 v[2:3], v[64:65], v[34:35], v[2:3] op_sel_hi:[0,1,1]\n\tv_pk_fma_f32 v[4:5], v[64:65], v[36:37], v[4:5] op_sel_hi:[0,1,1]\n\t"
          "v_pk_fma_f32 v[6:7], v[64:65], v[38:39], v[6:7] op_sel_hi:[0,1,1]\n\tv_pk_fma_f32 v[8:9], v[64:65], v[40:41], v[8:9] op_sel_hi:[0,1,1]\n\tv_pk_fma_f32 v[10:11], v[64:65], v[42:43], v[10:11] op_sel_hi:[0,1,1]\n\tv_pk_fma_f32 v[12:13], v[64:65], v[44:45], v[12:13] op_sel_hi:[0,1,1]\n\t"
          "v_pk_fma_f32 v[14:15], v[64:65], v[46:47], v[14:15] op_sel_hi:[0,1,1]\n\tv_pk_fma_f32 v[0:1], v[64:65], v[0:1], v[48:49] op_sel:[1,0,0]\n\tv_pk_fma_f32 v[2:3], v[64:65], v[2:3], v[50:51] op_sel:[1,0,0]\n\tv_pk_fma_f32 v[4:5], v[64:65], v[4:5], v[52:53] op_sel:[1,0,0]\n\t"
          "v_pk_fma_f32 v[6:7], v[64:65], v[6:7], v[54:55] op_sel:[1,0,0]\n\tv_pk_fma_f32 v[8:9], v[64:65], v[8:9], v[56:57] op_sel:[1,0,0]\n\tv_pk_fma_f32 v[10:11], v[64:65], v[10:11], v[58:59] op_sel:[1,0,0]\n\tv_pk_fma_f32 v[12:13], v[64:65], v[12:13], v[60:61] op_sel:[1,0,0]\n\t"
          "v_pk_fma_f32 v[14:15], v[64:65], v[14:15], v[62:63] op_sel:[1,0,0]\n\tv_pk_fma_f32 v[68:69], v[0:1], v[0:1], v[84:85] op_sel_hi:[1,1,0] neg_lo:[1,0,0] neg_hi:[1,0,0]\n\tv_pk_fma_f32 v[70:71], v[2:3], v[2:3], v[84:85] op_sel_hi:[1,1,0] neg_lo:[1,0,0] neg_hi:[1,0,0]\n\tv_pk_fma_f32 v[72:73], v[4:5], v[4:5], v[84:85] op_sel_hi:[1,1,0] neg_lo:[1,0,0] neg_hi:[1,0,0]\n\t"
          "v_pk_fma_f32 v[74:75], v[6:7], v[6:7], v[84:85] op_sel_hi:[1,1,0] neg_lo:[1,0,0] neg_hi:[1,0,0]\n\tv_pk_fma_f32 v[76:77], v[8:9], v[8:9], v[84:85] op_sel_hi:[1,1,0] neg_lo:[1,0,0] neg_hi:[1,0,0]\n\tv_pk_fma_f32 v[78:79], v[10:11], v[10:11], v[84:85] op_sel_hi:[1,1,0] neg_lo:[1,0,0] neg_hi:[1,0,0]\n\tv_pk_fma_f32 v[80:81], v[12:13], v[12:13], v[84:85] op_sel_hi:[1,1,0] neg_lo:[1,0,0] neg_hi:[1,0,0]\n\t"
          "v_pk_fma_f32 v[82:83], v[14:15], v[14:15], v[84:85] op_sel_hi:[1,1,0] neg_lo:[1,0,0] neg_hi:[1,0,0]\n\tv_pk_mul_f32 v[68:69], v[68:69], v[0:1]\n\tv_pk_mul_f32 v[70:71], v[70:71], v[2:3]\n\tv_pk_mul_f32 v[72:73], v[72:73], v[4:5]\n\t"
          "v_pk_mul_f32 v[74:75], v[74:75], v[6:7]\n\tv_pk_mul_f32 v[76:77], v[76:77], v[8:9]\n\tv_pk_mul_f32 v[78:79], v[78:79], v[10:11]\n\tv_pk_mul_f32 v[80:81], v[80:81], v[12:13]\n\t"
          "v_pk_mul_f32 v[82:83], v[82:83], v[14:15]\n\tv_exp_f32 v68, v68\n\tv_exp_f32 v69, v69\n\tv_exp_f32 v70, v70\n\t"
          "v_exp_f32 v71, v71\n\tv_exp_f32 v72, v72\n\tv_exp_f32 v73, v73\n\tv_exp_f32 v74, v74\n\t"
          "v_exp_f32 v75, v75\n\tv_exp_f32 v76, v76\n\tv_exp_f32 v77, v77\n\tv_exp_f32 v78, v78\n\t"
          "v_exp_f32 v79, v79\n\tv_exp_f32 v80, v80\n\tv_exp_f32 v81, v81\n\tv_exp_f32 v82, v82\n\t"
          "v_exp_f32 v83, v83\n\tv_pk_fma_f32 v[68:69], v[68:69], v[84:85], v[84:85] op_sel:[0,1,1]\n\tv_pk_fma_f32 v[70:71], v[70:71], v[84:85], v[84:85] op_sel:[0,1,1]\n\tv_pk_fma_f32 v[72:73], v[72:73], v[84:85], v[84:85] op_sel:[0,1,1]\n\t"
          "v_pk_fma_f32 v[74:75], v[74:75], v[84:85], v[84:85] op_sel:[0,1,1]\n\tv_pk_fma_f32 v[76:77], v[76:77], v[84:85], v[84:85] op_sel:[0,1,1]\n\tv_pk_fma_f32 v[78:79], v[78:79], v[84:85], v[84:85] op_sel:[0,1,1]\n\tv_pk_fma_f32 v[80:81], v[80:81], v[84:85], v[84:85] op_sel:[0,1,1]\n\t"
          "v_pk_fma_f32 v[82:83], v[82:83], v[84:85], v[84:85] op_sel:[0,1,1]\n\tv_rcp_f32 v68, v68\n\tv_rcp_f32 v69, v69\n\tv_rcp_f32 v70, v70\n\t"
          "v_rcp_f32 v71, v71\n\tv_rcp_f32 v72, v72\n\tv_rcp_f32 v73, v73\n\tv_rcp_f32 v74, v74\n\t"
          "v_rcp_f32 v75, v75\n\tv_rcp_f32 v76, v76\n\tv_rcp_f32 v77, v77\n\tv_rcp_f32 v78, v78\n\t"
          "v_rcp_f32 v79, v79\n\tv_rcp_f32 v80, v80\n\tv_rcp_f32 v81, v81\n\tv_rcp_f32 v82, v82\n\t"
          "v_rcp_f32 v83, v83\n\tv_pk_mul_f32 v[68:69], v[68:69], v[0:1]\n\tv_pk_mul_f32 v[70:71], v[70:71], v[2:3]\n\tv_pk_mul_f32 v[72:73], v[72:73], v[4:5]\n\t"
          "v_pk_mul_f32 v[74:75], v[74:75], v[6:7]\n\tv_pk_mul_f32 v[76:77], v[76:77], v[8:9]\n\tv_pk_mul_f32 v[78:79], v[78:79], v[10:11]\n\tv_pk_mul_f32 v[80:81], v[80:81], v[12:13]\n\t"
          "v_pk_mul_f32 v[82:83], v[82:83], v[14:15]\n\tv_cvt_pk_bf16_f32 v96, v68, v69\n\tv_cvt_pk_bf16_f32 v97, v70, v71\n\tv_cvt_pk_bf16_f32 v98, v72, v73\n\t"
          "v_cvt_pk_bf16_f32 v99, v74, v75\n\tv_cvt_pk_bf16_f32 v104, v76, v77\n\tv_cvt_pk_bf16_f32 v105, v78, v79\n\tv_cvt_pk_bf16_f32 v106, v80, v81\n\t"
          "v_cvt_pk_bf16_f32 v107, v82, v83\n\tbuffer_store_dwordx4 v[96:99], %[l16], %[desc], %[sh] offen offset:0 sc1\n\tbuffer_store_dwordx4 v[104:107], %[l16], %[desc], %[sh] offen offset:2048 sc1\n\tv_subrev_u32 v114, 1536, %[vtab]\n\t"
          "v_mov_b32 v112, %[mef]\n\tv_mov_b32 v113, %[lay1]\n\tds_read_b128 v[32:35], v114 offset:128\n\tds_read_b128 v[36:39], v114 offset:160\n\t"
          "ds_read_b128 v[40:43], v114 offset:192\n\tds_read_b128 v[44:47], v114 offset:224\n\tds_read_b128 v[48:51], v114 offset:256\n\tds_read_b128 v[52:55], v114 offset:288\n\t"
          "ds_read_b128 v[56:59], v114 offset:320\n\tds_read_b128 v[60:63], v114 offset:352\n\ts_setprio 0\n\tMW%=:\n\t"
          "ds_read_b32 v115, v112\n\ts_waitcnt lgkmcnt(0)\n\tv_cmp_ne_u32 vcc, v113, v115\n\ts_cbranch_vccnz MW%=\n\t"
          "s_setprio 3\n\tds_read_b128 v[16:19], %[vex] offset:0\n\tds_read_b128 v[20:23], %[vex] offset:1024\n\tds_read_b128 v[24:27], %[vex] offset:2048\n\t"
          "ds_read_b128 v[28:31], %[vex] offset:3072\n\ts_waitcnt lgkmcnt(0)\n\tv_pk_fma_f32 v[16:17], v[66:67], v[32:33], v[16:17] op_sel_hi:[0,1,1]\n\tv_pk_fma_f32 v[18:19], v[66:67], v[34:35], v[18:19] op_sel_hi:[0,1,1]\n\t"
          "v_pk_fma_f32 v[20:21], v[66:67], v[36:37], v[20:21] op_sel_hi:[0,1,1]\n\tv_pk_fma_f32 v[22:23], v[66:67], v[38:39], v[22:23] op_sel_hi:[0,1,1]\n\tv_pk_fma_f32 v[24:25], v[66:67], v[40:41], v[24:25] op_sel_hi:[0,1,1]\n\tv_pk_fma_f32 v[26:27], v[66:67], v[42:43], v[26:27] op_sel_hi:[0,1,1]\n\t"
          "v_pk_fma_f32 v[28:29], v[66:67], v[44:45], v[28:29] op_sel_hi:[0,1,1]\n\tv_pk_fma_f32 v[30:31], v[66:67], v[46:47], v[30:31] op_sel_hi:[0,1,1]\n\tv_pk_fma_f32 v[16:17], v[66:67], v[16:17], v[48:49] op_sel:[1,0,0]\n\tv_pk_fma_f32 v[18:19], v[66:67], v[18:19], v[50:51] op_sel:[1,0,0]\n\t"
          "v_pk_fma_f32 v[20:21], v[66:67], v[20:21], v[52:53] op_sel:[1,0,0]\n\tv_pk_fma_f32 v[22:23], v[66:67], v[22:23], v[54:55] op_sel:[1,0,0]\n\tv_pk_fma_f32 v[24:25], v[66:67], v[24:25], v[56:57] op_sel:[1,0,0]\n\tv_pk_fma_f32 v[26:27], v[66:67], v[26:27], v[58:59] op_sel:[1,0,0]\n\t"
          "v_pk_fma_f32 v[28:29], v[66:67], v[28:29], v[60:61] op_sel:[1,0,0]\n\tv_pk_fma_f32 v[30:31], v[66:67], v[30:31], v[62:63] op_sel:[1,0,0]\n\tv_pk_fma_f32 v[68:69], v[16:17], v[16:17], v[84:85] op_sel_hi:[1,1,0] neg_lo:[1,0,0] neg_hi:[1,0,0]\n\tv_pk_fma_f32 v[70:71], v[18:19], v[18:19], v[84:85] op_sel_hi:[1,1,0] neg_lo:[1,0,0] neg_hi:[1,0,0]\n\t"
          "v_pk_fma_f32 v[72:73], v[20:21], v[20:21], v[84:85] op_sel_hi:[1,1,0] neg_lo:[1,0,0] neg_hi:[1,0,0]\n\tv_pk_fma_f32 v[74:75], v[22:23], v[22:23], v[84:85] op_sel_hi:[1,1,0] neg_lo:[1,0,0] neg_hi:[1,0,0]\n\tv_pk_fma_f32 v[76:77], v[24:25], v[24:25], v[84:85] op_sel_hi:[1,1,0] neg_lo:[1,0,0] neg_hi:[1,0,0]\n\tv_pk_fma_f32 v[78:79], v[26:27], v[26:27], v[84:85] op_sel_hi:[1,1,0] neg_lo:[1,0,0] neg_hi:[1,0,0]\n\t"
          "v_pk_fma_f32 v[80:81], v[28:29], v[28:29], v[84:85] op_sel_hi:[1,1,0] neg_lo:[1,0,0] neg_hi:[1,0,0]\n\tv_pk_fma_f32 v[82:83], v[30:31], v[30:31], v[84:85] op_sel_hi:[1,1,0] neg_lo:[1,0,0] neg_hi:[1,0,0]\n\tv_pk_mul_f32 v[68:69], v[68:69], v[16:17]\n\tv_pk_mul_f32 v[70:71], v[70:71], v[18:19]\n\t"
          "v_pk_mul_f32 v[72:73], v[72:73], v[20:21]\n\tv_pk_mul_f32 v[74:75], v[74:75], v[22:23]\n\tv_pk_mul_f32 v[76:77], v[76:77], v[24:25]\n\tv_pk_mul_f32 v[78:79], v[78:79], v[26:27]\n\t"
          "v_pk_mul_f32 v[80:81], v[80:81], v[28:29]\n\tv_pk_mul_f32 v[82:83], v[82:83], v[30:31]\n\tv_exp_f32 v68, v68\n\tv_exp_f32 v69, v69\n\t"
          "v_exp_f32 v70, v70\n\tv_exp_f32 v71, v71\n\tv_exp_f32 v72, v72\n\tv_exp_f32 v73, v73\n\t"
          "v_exp_f32 v74, v74\n\tv_exp_f32 v75, v75\n\tv_exp_f32 v76, v76\n\tv_exp_f32 v77, v77\n\t"
          "v_exp_f32 v78, v78\n\tv_exp_f32 v79, v79\n\tv_exp_f32 v80, v80\n\tv_exp_f32 v81, v81\n\t"
          "v_exp_f32 v82, v82\n\tv_exp_f32 v83, v83\n\tv_pk_fma_f32 v[68:69], v[68:69], v[84:85], v[84:85] op_sel:[0,1,1]\n\tv_pk_fma_f32 v[70:71], v[70:71], v[84:85], v[84:85] op_sel:[0,1,1]\n\t"
          "v_pk_fma_f32 v[72:73], v[72:73], v[84:85], v[84:85] op_sel:[0,1,1]\n\tv_pk_fma_f32 v[74:75], v[74:75], v[84:85], v[84:85] op_sel:[0,1,1]\n\tv_pk_fma_f32 v[76:77], v[76:77], v[84:85], v[84:85] op_sel:[0,1,1]\n\tv_pk_fma_f32 v[78:79], v[78:79], v[84:85], v[84:85] op_sel:[0,1,1]\n\t"
          "v_pk_fma_f32 v[80:81], v[80:81], v[84:85], v[84:85] op_sel:[0,1,1]\n\tv_pk_fma_f32 v[82:83], v[82:83], v[84:85], v[84:85] op_sel:[0,1,1]\n\tv_rcp_f32 v68, v68\n\tv_rcp_f32 v69, v69\n\t"
          "v_rcp_f32 v70, v70\n\tv_rcp_f32 v71, v71\n\tv_rcp_f32 v72, v72\n\tv_rcp_f32 v73, v73\n\t"
          "v_rcp_f32 v74, v74\n\tv_rcp_f32 v75, v75\n\tv_rcp_f32 v76, v76\n\tv_rcp_f32 v77, v77\n\t"
          "v_rcp_f32 v78, v78\n\tv_rcp_f32 v79, v79\n\tv_rcp_f32 v80, v80\n\tv_rcp_f32 v81, v81\n\t"
          "v_rcp_f32 v82, v82\n\tv_rcp_f32 v83, v83\n\tv_pk_mul_f32 v[68:69], v[68:69], v[16:17]\n\tv_pk_mul_f32 v[70:71], v[70:71], v[18:19]\n\t"
          "v_pk_mul_f32 v[72:73], v[72:73], v[20:21]\n\tv_pk_mul_f32 v[74:75], v[74:75], v[22:23]\n\tv_pk_mul_f32 v[76:77], v[76:77], v[24:25]\n\tv_pk_mul_f32 v[78:79], v[78:79], v[26:27]\n\t"
          "v_pk_mul_f32 v[80:81], v[80:81], v[28:29]\n\tv_pk_mul_f32 v[82:83], v[82:83], v[30:31]\n\tv_cvt_pk_bf16_f32 v100, v68, v69\n\tv_cvt_pk_bf16_f32 v101, v70, v71\n\t"
          "v_cvt_pk_bf16_f32 v102, v72, v73\n\tv_cvt_pk_bf16_f32 v103, v74, v75\n\tv_cvt_pk_bf16_f32 v108, v76, v77\n\tv_cvt_pk_bf16_f32 v109, v78, v79\n\t"
          "v_cvt_pk_bf16_f32 v110, v80, v81\n\tv_cvt_pk_bf16_f32 v111, v82, v83\n\tbuffer_store_dwordx4 v[100:103], %[l16], %[desc], %[sh2] offen offset:1024 sc1\n\tbuffer_store_dwordx4 v[108:111], %[l16], %[desc], %[sh2] offen offset:3072 sc1\n\t"
          "ds_write_b128 %[vex], v[100:103]\n\tds_write_b128 %[vex], v[108:111] offset:1024\n\tME%=:\n\ts_setprio 0\n\t"
          "v_mov_b64 v[68:69], %[tag]\n\tv_mov_b64 v[70:71], %[tag]\n\tv_lshlrev_b32 v72, 3, %[l16]\n\ts_mov_b32 m0, %[mdu]\n\t"
          "s_waitcnt vmcnt(0) lgkmcnt(0)\n\ts_barrier\n\ts_cmp_lg_u32 %[w], 0\n\ts_cbranch_scc1 MQ%=\n\t"
          "s_mov_b64 exec, 0xff\n\tbuffer_store_dwordx4 v[68:71], %[l16], %[desc], %[sf] offen sc1\n\ts_mov_b64 exec, -1\n\ts_nop 1\n\t"
          "MQ%=:\n\ts_nop 7\n\ts_cmp_ge_u32 %[w], 4\n\ts_cbranch_scc1 MT%=\n\t"
          "buffer_load_dwordx4 v[224:227], %[l16], %[desc], %[sw] offen offset:0 sc1\n\tbuffer_load_dwordx4 v[228:231], %[l16], %[desc], %[sw] offen offset:1024 sc1\n\tbuffer_load_dwordx4 v[232:235], %[l16], %[desc], %[sw] offen offset:2048 sc1\n\tbuffer_load_dwordx4 v[236:239], %[l16], %[desc], %[sw] offen offset:3072 sc1\n\t"
          "s_add_u32 %[sw], %[sw], 4096\n\tbuffer_load_dwordx4 v[240:243], %[l16], %[desc], %[sw] offen offset:0 sc1\n\tbuffer_load_dwordx4 v[244:247], %[l16], %[desc], %[sw] offen offset:1024 sc1\n\tbuffer_load_dwordx4 v[248:251], %[l16], %[desc], %[sw] offen offset:2048 sc1\n\t"
          "buffer_load_dwordx4 v[252:255], %[l16], %[desc], %[sw] offen offset:3072 sc1\n\ts_branch MU%=\n\tMT%=:\n\ts_cmp_ge_u32 %[w], 6\n\t"
          "s_cbranch_scc1 MV%=\n\tds_read_b128 v[100:103], %[vex]\n\tds_read_b128 v[108:111], %[vex] offset:1024\n\tbuffer_load_dwordx4 v[216:219], %[l16], %[desc], %[sw] offen offset:2048 sc1\n\t"
          "buffer_load_dwordx4 v[220:223], %[l16], %[desc], %[sw] offen offset:3072 sc1\n\ts_add_u32 %[sw], %[sw], 4096\n\tbuffer_load_dwordx4 v[224:227], %[l16], %[desc], %[sw] offen offset:0 sc1\n\tbuffer_load_dwordx4 v[228:231], %[l16], %[desc], %[sw] offen offset:1024 sc1\n\t"
          "buffer_load_dwordx4 v[232:235], %[l16], %[descq], %[sq] offen offset:0 sc1\n\tbuffer_load_dwordx4 v[236:239], %[l16], %[descq], %[sq] offen offset:1024 sc1\n\tbuffer_load_dwordx4 v[240:243], %[l16], %[descq], %[sq] offen offset:2048 sc1\n\tbuffer_load_dwordx4 v[244:247], %[l16], %[descq], %[sq] offen offset:3072 sc1\n\t"
          "s_add_u32 %[sq], %[sq], 4096\n\tbuffer_load_dwordx4 v[248:251], %[l16], %[descq], %[sq] offen offset:0 sc1\n\tbuffer_load_dwordx4 v[252:255], %[l16], %[descq], %[sq] offen offset:1024 sc1\n\ts_waitcnt lgkmcnt(0)\n\t"
          "s_branch MU%=\n\tMV%=:\n\tbuffer_load_dwordx4 v[232:235], %[l16], %[descq], %[sq] offen offset:0 sc1\n\tbuffer_load_dwordx4 v[236:239], %[l16], %[descq], %[sq] offen offset:1024 sc1\n\t"
          "buffer_load_dwordx4 v[240:243], %[l16], %[descq], %[sq] offen offset:2048 sc1\n\tbuffer_load_dwordx4 v[244:247], %[l16], %[descq], %[sq] offen offset:3072 sc1\n\ts_add_u32 %[sq], %[sq], 4096\n\tbuffer_load_dwordx4 v[248:251], %[l16], %[descq], %[sq] offen offset:0 sc1\n\t"
          "buffer_load_dwordx4 v[252:255], %[l16], %[descq], %[sq] offen offset:1024 sc1\n\tMU%=:\n\t"
          : "=&{v[96:111]}"(hp), [sx] "+s"(sx), [sw] "+s"(sw), [sq] "+s"(sq), [bud] "+s"(bud)
          : [l16] "v"(l16), [vx0] "v"(lds0 + L_XRES + t0 * 1024 + l16), [vr] "v"(lds0 + L_ACT + t0 * 32768 + l16), [vs] "v"(lds0 + L_STAT + w * 512 + tok * 16 + h * 8),
            [vst] "v"(lds0 + L_STAT + tok * 16), [vtab] "v"(ltab + h * 16), [vf] "v"(flag_off(SL_X2 + sl, c * 8 + mm) + (lane >> 6)),
            [vex] "v"(lds0 + 131072 + (w & 1) * 4096 + l16), [mef] "s"(US(lds0 + L_FLAG + 16 + (w & 1) * 4)), [lay1] "s"(layer + 1),
            [sh2] "s"(OFF_L0 + LO_H + ((c * 8 + j) * 28 + 16 + 4 * (w & 1)) * 1024),
            [desc] "s"(desc), [descq] "s"(mkdesc(ws, layer == 0 ? WS_BYTES : 0u)), [ss] "s"(OFF_L0 + LO_S2 + (c * 8 + mm) * 2048), [w] "s"(w), [tag] "s"(tag),
            [ma] "s"(US(lds0 + L_ACT + w * 4096)), [sh] "s"(OFF_L0 + LO_H + ((c * 8 + j) * 28 + 4 * ntl + t0) * 1024), [sf] "s"(flag_off(SL_H + sl, c * 8 + j)), [mdu] "s"(US(lds0 + L_DUMMY))
          : "v0", "v1", "v2", "v3", "v4", "v5", "v6", "v7", "v8", "v9", "v10", "v11", "v12", "v13", "v14", "v15", "v16", "v17", "v18", "v19", "v20", "v21", "v22", "v23", "v24", "v25", "v26", "v27", "v28", "v29", "v30", "v31", "v32", "v33", "v34", "v35", "v36", "v37", "v38", "v39", "v40", "v41", "v42", "v43", "v44", "v45", "v46", "v47", "v48", "v49", "v50", "v51", "v52", "v53", "v54", "v55", "v56", "v57", "v58", "v59", "v60", "v61", "v62", "v63", "v64", "v65", "v66", "v67", "v68", "v69", "v70", "v71", "v72", "v73", "v74", "v75", "v76", "v77", "v78", "v79", "v80", "v81", "v82", "v83", "v84", "v85", "v112", "v113", "v114", "v115", "v116", "v117", "v118", "v119", "v120", "v121", "v122", "v123", "v124", "v125", "v126", "v127", "scc", "vcc", "memory");
    }
    {
      PHASE_VARS();
      if (layer == 0)
        asm volatile("s_mov_b32 m0, %3\n\ts_mov_b32 exec_hi, 0\n\ts_nop 3\n\tbuffer_load_dword %0, %1, %2 offen sc1 lds\n\ts_mov_b32 exec_hi, -1"
                     :: "v"(lane * 4), "s"(desc), "s"(US(OFF_CSQ + qkv_tile(w, j) * 128)), "s"(US(ltab)) : "memory");
      u32 sq = OFF_WQKV + qkv_tile(w, j) * 32768, t0;
      asm volatile(
          "s_nop 4\n\ts_cmp_ge_u32 %[w], 6\n\ts_cbranch_scc1 H0%=\n\tv_mfma_f32_32x32x16_bf16 v[0:15], v[128:131], v[96:99], 0\n\t"
          "v_mfma_f32_32x32x16_bf16 v[32:47], v[132:135], v[96:99], 0\n\tv_mfma_f32_32x32x16_bf16 v[16:31], v[128:131], v[100:103], 0\n\tv_mfma_f32_32x32x16_bf16 v[48:63], v[132:135], v[100:103], 0\n\tv_mfma_f32_32x32x16_bf16 v[0:15], v[136:139], v[104:107], v[0:15]\n\t"
          "v_mfma_f32_32x32x16_bf16 v[32:47], v[140:143], v[104:107], v[32:47]\n\tv_mfma_f32_32x32x16_bf16 v[16:31], v[136:139], v[108:111], v[16:31]\n\tv_mfma_f32_32x32x16_bf16 v[48:63], v[140:143], v[108:111], v[48:63]\n\ts_cmp_ge_u32 %[w], 4\n\t"
          "s_cbranch_scc1 H0%=\n\tH0%=:\n\tHPA%=:\n\tbuffer_load_dwordx2 v[64:65], %[vf], %[desc], 0 offen sc1\n\t"
          "s_waitcnt vmcnt(0)\n\tv_cmp_ne_u64 vcc, %[tag], v[64:65]\n\ts_cbranch_vccz HPB%=\n\ts_sub_u32 %[bud], %[bud], 1\n\t"
          "s_cbranch_scc0 HPA%=\n\ts_mov_b32 %[bud], 0\n\tHPB%=:\n\ts_cmp_ge_u32 %[w], 4\n\t"
          "s_cbranch_scc1 H4%=\n\ts_add_u32 %[t0], %[j], 1\n\ts_and_b32 %[t0], %[t0], 7\n\ts_mul_i32 %[t0], %[t0], 28672\n\t"
          "s_add_u32 %[t0], %[t0], %[shb]\n\ts_add_u32 %[t0], %[t0], %[sk]\n\tbuffer_load_dwordx4 v[112:115], %[l16], %[desc], %[t0] offen offset:0 sc1\n\tbuffer_load_dwordx4 v[116:119], %[l16], %[desc], %[t0] offen offset:1024 sc1\n\t"
          "buffer_load_dwordx4 v[120:123], %[l16], %[desc], %[t0] offen offset:2048 sc1\n\tbuffer_load_dwordx4 v[124:127], %[l16], %[desc], %[t0] offen offset:3072 sc1\n\ts_add_u32 %[t0], %[j], 2\n\ts_and_b32 %[t0], %[t0], 7\n\t"
          "s_mul_i32 %[t0], %[t0], 28672\n\ts_add_u32 %[t0], %[t0], %[shb]\n\ts_add_u32 %[t0], %[t0], %[sk]\n\tbuffer_load_dwordx4 v[64:67], %[l16], %[desc], %[t0] offen offset:0 sc1\n\t"
          "buffer_load_dwordx4 v[68:71], %[l16], %[desc], %[t0] offen offset:1024 sc1\n\tbuffer_load_dwordx4 v[72:75], %[l16], %[desc], %[t0] offen offset:2048 sc1\n\tbuffer_load_dwordx4 v[76:79], %[l16], %[desc], %[t0] offen offset:3072 sc1\n\ts_add_u32 %[t0], %[j], 3\n\t"
          "s_and_b32 %[t0], %[t0], 7\n\ts_mul_i32 %[t0], %[t0], 28672\n\ts_add_u32 %[t0], %[t0], %[shb]\n\ts_add_u32 %[t0], %[t0], %[sk]\n\t"
          "buffer_load_dwordx4 v[80:83], %[l16], %[desc], %[t0] offen offset:0 sc1\n\tbuffer_load_dwordx4 v[96:99], %[l16], %[desc], %[t0] offen offset:1024 sc1\n\tbuffer_load_dwordx4 v[100:103], %[l16], %[desc], %[t0] offen offset:2048 sc1\n\tbuffer_load_dwordx4 v[104:107], %[l16], %[desc], %[t0] offen offset:3072 sc1\n\t"
          "s_add_u32 %[t0], %[j], 4\n\ts_and_b32 %[t0], %[t0], 7\n\ts_mul_i32 %[t0], %[t0], 28672\n\ts_add_u32 %[t0], %[t0], %[shb]\n\t"
          "s_add_u32 %[t0], %[t0], %[sk]\n\tbuffer_load_dwordx4 v[108:111], %[l16], %[desc], %[t0] offen offset:0 sc1\n\ts_waitcnt vmcnt(12)\n\tv_mfma_f32_32x32x16_bf16 v[0:15], v[144:147], v[112:115], v[0:15]\n\t"
          "v_mfma_f32_32x32x16_bf16 v[32:47], v[148:151], v[112:115], v[32:47]\n\tbuffer_load_dwordx4 v[112:115], %[l16], %[desc], %[t0] offen offset:1024 sc1\n\ts_waitcnt vmcnt(12)\n\tv_mfma_f32_32x32x16_bf16 v[16:31], v[144:147], v[116:119], v[16:31]\n\t"
          "v_mfma_f32_32x32x16_bf16 v[48:63], v[148:151], v[116:119], v[48:63]\n\tbuffer_load_dwordx4 v[116:119], %[l16], %[desc], %[t0] offen offset:2048 sc1\n\ts_waitcnt vmcnt(12)\n\tv_mfma_f32_32x32x16_bf16 v[0:15], v[152:155], v[120:123], v[0:15]\n\t"
          "v_mfma_f32_32x32x16_bf16 v[32:47], v[156:159], v[120:123], v[32:47]\n\tbuffer_load_dwordx4 v[120:123], %[l16], %[desc], %[t0] offen offset:3072 sc1\n\ts_waitcnt vmcnt(12)\n\tv_mfma_f32_32x32x16_bf16 v[16:31], v[152:155], v[124:127], v[16:31]\n\t"
          "v_mfma_f32_32x32x16_bf16 v[48:63], v[156:159], v[124:127], v[48:63]\n\ts_add_u32 %[t0], %[j], 5\n\ts_and_b32 %[t0], %[t0], 7\n\ts_mul_i32 %[t0], %[t0], 28672\n\t"
          "s_add_u32 %[t0], %[t0], %[shb]\n\ts_add_u32 %[t0], %[t0], %[sk]\n\tbuffer_load_dwordx4 v[124:127], %[l16], %[desc], %[t0] offen offset:0 sc1\n\ts_waitcnt vmcnt(12)\n\t"
          "v_mfma_f32_32x32x16_bf16 v[0:15], v[160:163], v[64:67], v[0:15]\n\tv_mfma_f32_32x32x16_bf16 v[32:47], v[164:167], v[64:67], v[32:47]\n\tbuffer_load_dwordx4 v[64:67], %[l16], %[desc], %[t0] offen offset:1024 sc1\n\ts_waitcnt vmcnt(12)\n\t"
          "v_mfma_f32_32x32x16_bf16 v[16:31], v[160:163], v[68:71], v[16:31]\n\tv_mfma_f32_32x32x16_bf16 v[48:63], v[164:167], v[68:71], v[48:63]\n\tbuffer_load_dwordx4 v[68:71], %[l16], %[desc], %[t0] offen offset:2048 sc1\n\ts_waitcnt vmcnt(12)\n\t"
          "v_mfma_f32_32x32x16_bf16 v[0:15], v[168:171], v[72:75], v[0:15]\n\tv_mfma_f32_32x32x16_bf16 v[32:47], v[172:175], v[72:75], v[32:47]\n\tbuffer_load_dwordx4 v[72:75], %[l16], %[desc], %[t0] offen offset:3072 sc1\n\ts_waitcnt vmcnt(12)\n\t"
          "v_mfma_f32_32x32x16_bf16 v[16:31], v[168:171], v[76:79], v[16:31]\n\tv_mfma_f32_32x32x16_bf16 v[48:63], v[172:175], v[76:79], v[48:63]\n\ts_add_u32 %[t0], %[j], 6\n\ts_and_b32 %[t0], %[t0], 7\n\t"
          "s_mul_i32 %[t0], %[t0], 28672\n\ts_add_u32 %[t0], %[t0], %[shb]\n\ts_add_u32 %[t0], %[t0], %[sk]\n\tbuffer_load_dwordx4 v[76:79], %[l16], %[desc], %[t0] offen offset:0 sc1\n\t"
          "s_waitcnt vmcnt(12)\n\tv_mfma_f32_32x32x16_bf16 v[0:15], v[176:179], v[80:83], v[0:15]\n\tv_mfma_f32_32x32x16_bf16 v[32:47], v[180:183], v[80:83], v[32:47]\n\tbuffer_load_dwordx4 v[80:83], %[l16], %[desc], %[t0] offen offset:1024 sc1\n\t"
          "s_waitcnt vmcnt(12)\n\tv_mfma_f32_32x32x16_bf16 v[16:31], v[176:179], v[96:99], v[16:31]\n\tv_mfma_f32_32x32x16_bf16 v[48:63], v[180:183], v[96:99], v[48:63]\n\tbuffer_load_dwordx4 v[96:99], %[l16], %[desc], %[t0] offen offset:2048 sc1\n\t"
          "s_waitcnt vmcnt(12)\n\tv_mfma_f32_32x32x16_bf16 v[0:15], v[184:187], v[100:103], v[0:15]\n\tv_mfma_f32_32x32x16_bf16 v[32:47], v[188:191], v[100:103], v[32:47]\n\tbuffer_load_dwordx4 v[100:103], %[l16], %[desc], %[t0] offen offset:3072 sc1\n\t"
          "s_waitcnt vmcnt(12)\n\tv_mfma_f32_32x32x16_bf16 v[16:31], v[184:187], v[104:107], v[16:31]\n\tv_mfma_f32_32x32x16_bf16 v[48:63], v[188:191], v[104:107], v[48:63]\n\ts_add_u32 %[t0], %[j], 7\n\t"
          "s_and_b32 %[t0], %[t0], 7\n\ts_mul_i32 %[t0], %[t0], 28672\n\ts_add_u32 %[t0], %[t0], %[shb]\n\ts_add_u32 %[t0], %[t0], %[sk]\n\t"
          "buffer_load_dwordx4 v[104:107], %[l16], %[desc], %[t0] offen offset:0 sc1\n\ts_waitcnt vmcnt(12)\n\tv_mfma_f32_32x32x16_bf16 v[0:15], v[192:195], v[108:111], v[0:15]\n\tv_mfma_f32_32x32x16_bf16 v[32:47], v[196:199], v[108:111], v[32:47]\n\t"
          "buffer_load_dwordx4 v[108:111], %[l16], %[desc], %[t0] offen offset:1024 sc1\n\ts_waitcnt vmcnt(12)\n\tv_mfma_f32_32x32x16_bf16 v[16:31], v[192:195], v[112:115], v[16:31]\n\tv_mfma_f32_32x32x16_bf16 v[48:63], v[196:199], v[112:115], v[48:63]\n\t"
          "buffer_load_dwordx4 v[112:115], %[l16], %[desc], %[t0] offen offset:2048 sc1\n\ts_waitcnt vmcnt(12)\n\tv_mfma_f32_32x32x16_bf16 v[0:15], v[200:203], v[116:119], v[0:15]\n\tv_mfma_f32_32x32x16_bf16 v[32:47], v[204:207], v[116:119], v[32:47]\n\t"
          "buffer_load_dwordx4 v[116:119], %[l16], %[desc], %[t0] offen offset:3072 sc1\n\ts_waitcnt vmcnt(12)\n\tv_mfma_f32_32x32x16_bf16 v[16:31], v[200:203], v[120:123], v[16:31]\n\tv_mfma_f32_32x32x16_bf16 v[48:63], v[204:207], v[120:123], v[48:63]\n\t"
          "s_waitcnt vmcnt(11)\n\tv_mfma_f32_32x32x16_bf16 v[0:15], v[208:211], v[124:127], v[0:15]\n\tv_mfma_f32_32x32x16_bf16 v[32:47], v[212:215], v[124:127], v[32:47]\n\ts_waitcnt vmcnt(10)\n\t"
          "v_mfma_f32_32x32x16_bf16 v[16:31], v[208:211], v[64:67], v[16:31]\n\tv_mfma_f32_32x32x16_bf16 v[48:63], v[212:215], v[64:67], v[48:63]\n\ts_waitcnt vmcnt(9)\n\tv_mfma_f32_32x32x16_bf16 v[0:15], v[216:219], v[68:71], v[0:15]\n\t"
          "v_mfma_f32_32x32x16_bf16 v[32:47], v[220:223], v[68:71], v[32:47]\n\ts_waitcnt vmcnt(8)\n\tv_mfma_f32_32x32x16_bf16 v[16:31], v[216:219], v[72:75], v[16:31]\n\tv_mfma_f32_32x32x16_bf16 v[48:63], v[220:223], v[72:75], v[48:63]\n\t"
          "s_waitcnt vmcnt(7)\n\tv_mfma_f32_32x32x16_bf16 v[0:15], v[224:227], v[76:79], v[0:15]\n\tv_mfma_f32_32x32x16_bf16 v[32:47], v[228:231], v[76:79], v[32:47]\n\ts_waitcnt vmcnt(6)\n\t"
          "v_mfma_f32_32x32x16_bf16 v[16:31], v[224:227], v[80:83], v[16:31]\n\tv_mfma_f32_32x32x16_bf16 v[48:63], v[228:231], v[80:83], v[48:63]\n\ts_waitcnt vmcnt(5)\n\tv_mfma_f32_32x32x16_bf16 v[0:15], v[232:235], v[96:99], v[0:15]\n\t"
          "v_mfma_f32_32x32x16_bf16 v[32:47], v[236:239], v[96:99], v[32:47]\n\ts_waitcnt vmcnt(4)\n\tv_mfma_f32_32x32x16_bf16 v[16:31], v[232:235], v[100:103], v[16:31]\n\tv_mfma_f32_32x32x16_bf16 v[48:63], v[236:239], v[100:103], v[48:63]\n\t"
          "s_waitcnt vmcnt(3)\n\tv_mfma_f32_32x32x16_bf16 v[0:15], v[240:243], v[104:107], v[0:15]\n\tv_mfma_f32_32x32x16_bf16 v[32:47], v[244:247], v[104:107], v[32:47]\n\ts_waitcnt vmcnt(2)\n\t"
          "v_mfma_f32_32x32x16_bf16 v[16:31], v[240:243], v[108:111], v[16:31]\n\tv_mfma_f32_32x32x16_bf16 v[48:63], v[244:247], v[108:111], v[48:63]\n\ts_waitcnt vmcnt(1)\n\tv_mfma_f32_32x32x16_bf16 v[0:15], v[248:251], v[112:115], v[0:15]\n\t"
          "v_mfma_f32_32x32x16_bf16 v[32:47], v[252:255], v[112:115], v[32:47]\n\ts_waitcnt vmcnt(0)\n\tv_mfma_f32_32x32x16_bf16 v[16:31], v[248:251], v[116:119], v[16:31]\n\tv_mfma_f32_32x32x16_bf16 v[48:63], v[252:255], v[116:119], v[48:63]\n\t"
          "s_cmp_lg_u32 %[lay], 0\n\ts_cbranch_scc1 HE%=\n\tbuffer_load_dwordx4 v[128:131], %[l16], %[descq], %[sq] offen offset:0 sc1\n\tbuffer_load_dwordx4 v[132:135], %[l16], %[descq], %[sq] offen offset:1024 sc1\n\t"
          "buffer_load_dwordx4 v[136:139], %[l16], %[descq], %[sq] offen offset:2048 sc1\n\tbuffer_load_dwordx4 v[140:143], %[l16], %[descq], %[sq] offen offset:3072 sc1\n\ts_add_u32 %[sq], %[sq], 4096\n\tbuffer_load_dwordx4 v[144:147], %[l16], %[descq], %[sq] offen offset:0 sc1\n\t"
          "buffer_load_dwordx4 v[148:151], %[l16], %[descq], %[sq] offen offset:1024 sc1\n\tbuffer_load_dwordx4 v[152:155], %[l16], %[descq], %[sq] offen offset:2048 sc1\n\tbuffer_load_dwordx4 v[156:159], %[l16], %[descq], %[sq] offen offset:3072 sc1\n\ts_add_u32 %[sq], %[sq], 4096\n\t"
          "s_branch HE%=\n\tH4%=:\n\ts_cmp_lg_u32 %[w], 4\n\ts_cbranch_scc1 H5%=\n\t"
          "s_add_u32 %[t0], %[j], 1\n\ts_and_b32 %[t0], %[t0], 7\n\ts_mul_i32 %[t0], %[t0], 28672\n\ts_add_u32 %[t0], %[t0], %[shb]\n\t"
          "s_add_u32 %[t0], %[t0], 16384\n\tbuffer_load_dwordx4 v[112:115], %[l16], %[desc], %[t0] offen offset:0 sc1\n\tbuffer_load_dwordx4 v[116:119], %[l16], %[desc], %[t0] offen offset:1024 sc1\n\tbuffer_load_dwordx4 v[120:123], %[l16], %[desc], %[t0] offen offset:2048 sc1\n\t"
          "buffer_load_dwordx4 v[124:127], %[l16], %[desc], %[t0] offen offset:3072 sc1\n\ts_add_u32 %[t0], %[t0], 4096\n\tbuffer_load_dwordx4 v[64:67], %[l16], %[desc], %[t0] offen offset:0 sc1\n\tbuffer_load_dwordx4 v[68:71], %[l16], %[desc], %[t0] offen offset:1024 sc1\n\t"
          "buffer_load_dwordx4 v[72:75], %[l16], %[desc], %[t0] offen offset:2048 sc1\n\tbuffer_load_dwordx4 v[76:79], %[l16], %[desc], %[t0] offen offset:3072 sc1\n\ts_add_u32 %[t0], %[t0], 4096\n\tbuffer_load_dwordx4 v[80:83], %[l16], %[desc], %[t0] offen offset:0 sc1\n\t"
          "buffer_load_dwordx4 v[96:99], %[l16], %[desc], %[t0] offen offset:1024 sc1\n\tbuffer_load_dwordx4 v[100:103], %[l16], %[desc], %[t0] offen offset:2048 sc1\n\tbuffer_load_dwordx4 v[104:107], %[l16], %[desc], %[t0] offen offset:3072 sc1\n\ts_add_u32 %[t0], %[j], 2\n\t"
          "s_and_b32 %[t0], %[t0], 7\n\ts_mul_i32 %[t0], %[t0], 28672\n\ts_add_u32 %[t0], %[t0], %[shb]\n\ts_add_u32 %[t0], %[t0], 16384\n\t"
          "buffer_load_dwordx4 v[108:111], %[l16], %[desc], %[t0] offen offset:0 sc1\n\ts_waitcnt vmcnt(12)\n\tv_mfma_f32_32x32x16_bf16 v[0:15], v[144:147], v[112:115], v[0:15]\n\tv_mfma_f32_32x32x16_bf16 v[32:47], v[148:151], v[112:115], v[32:47]\n\t"
          "buffer_load_dwordx4 v[112:115], %[l16], %[desc], %[t0] offen offset:1024 sc1\n\ts_waitcnt vmcnt(12)\n\tv_mfma_f32_32x32x16_bf16 v[16:31], v[144:147], v[116:119], v[16:31]\n\tv_mfma_f32_32x32x16_bf16 v[48:63], v[148:151], v[116:119], v[48:63]\n\t"
          "buffer_load_dwordx4 v[116:119], %[l16], %[desc], %[t0] offen offset:2048 sc1\n\ts_waitcnt vmcnt(12)\n\tv_mfma_f32_32x32x16_bf16 v[0:15], v[152:155], v[120:123], v[0:15]\n\tv_mfma_f32_32x32x16_bf16 v[32:47], v[156:159], v[120:123], v[32:47]\n\t"
          "buffer_load_dwordx4 v[120:123], %[l16], %[desc], %[t0] offen offset:3072 sc1\n\ts_add_u32 %[t0], %[t0], 4096\n\ts_waitcnt vmcnt(12)\n\tv_mfma_f32_32x32x16_bf16 v[16:31], v[152:155], v[124:127], v[16:31]\n\t"
          "v_mfma_f32_32x32x16_bf16 v[48:63], v[156:159], v[124:127], v[48:63]\n\tbuffer_load_dwordx4 v[124:127], %[l16], %[desc], %[t0] offen offset:0 sc1\n\ts_waitcnt vmcnt(12)\n\tv_mfma_f32_32x32x16_bf16 v[0:15], v[160:163], v[64:67], v[0:15]\n\t"
          "v_mfma_f32_32x32x16_bf16 v[32:47], v[164:167], v[64:67], v[32:47]\n\tbuffer_load_dwordx4 v[64:67], %[l16], %[desc], %[t0] offen offset:1024 sc1\n\ts_waitcnt vmcnt(12)\n\tv_mfma_f32_32x32x16_bf16 v[16:31], v[160:163], v[68:71], v[16:31]\n\t"
          "v_mfma_f32_32x32x16_bf16 v[48:63], v[164:167], v[68:71], v[48:63]\n\tbuffer_load_dwordx4 v[68:71], %[l16], %[desc], %[t0] offen offset:2048 sc1\n\ts_waitcnt vmcnt(12)\n\tv_mfma_f32_32x32x16_bf16 v[0:15], v[168:171], v[72:75], v[0:15]\n\t"
          "v_mfma_f32_32x32x16_bf16 v[32:47], v[172:175], v[72:75], v[32:47]\n\tbuffer_load_dwordx4 v[72:75], %[l16], %[desc], %[t0] offen offset:3072 sc1\n\ts_add_u32 %[t0], %[t0], 4096\n\ts_waitcnt vmcnt(12)\n\t"
          "v_mfma_f32_32x32x16_bf16 v[16:31], v[168:171], v[76:79], v[16:31]\n\tv_mfma_f32_32x32x16_bf16 v[48:63], v[172:175], v[76:79], v[48:63]\n\tbuffer_load_dwordx4 v[76:79], %[l16], %[desc], %[t0] offen offset:0 sc1\n\ts_waitcnt vmcnt(12)\n\t"
          "v_mfma_f32_32x32x16_bf16 v[0:15], v[176:179], v[80:83], v[0:15]\n\tv_mfma_f32_32x32x16_bf16 v[32:47], v[180:183], v[80:83], v[32:47]\n\tbuffer_load_dwordx4 v[80:83], %[l16], %[desc], %[t0] offen offset:1024 sc1\n\ts_waitcnt vmcnt(12)\n\t"
          "v_mfma_f32_32x32x16_bf16 v[16:31], v[176:179], v[96:99], v[16:31]\n\tv_mfma_f32_32x32x16_bf16 v[48:63], v[180:183], v[96:99], v[48:63]\n\ts_waitcnt vmcnt(11)\n\tv_mfma_f32_32x32x16_bf16 v[0:15], v[184:187], v[100:103], v[0:15]\n\t"
          "v_mfma_f32_32x32x16_bf16 v[32:47], v[188:191], v[100:103], v[32:47]\n\ts_waitcnt vmcnt(10)\n\tv_mfma_f32_32x32x16_bf16 v[16:31], v[184:187], v[104:107], v[16:31]\n\tv_mfma_f32_32x32x16_bf16 v[48:63], v[188:191], v[104:107], v[48:63]\n\t"
          "s_waitcnt vmcnt(9)\n\tv_mfma_f32_32x32x16_bf16 v[0:15], v[192:195], v[108:111], v[0:15]\n\tv_mfma_f32_32x32x16_bf16 v[32:47], v[196:199], v[108:111], v[32:47]\n\ts_waitcnt vmcnt(8)\n\t"
          "v_mfma_f32_32x32x16_bf16 v[16:31], v[192:195], v[112:115], v[16:31]\n\tv_mfma_f32_32x32x16_bf16 v[48:63], v[196:199], v[112:115], v[48:63]\n\ts_waitcnt vmcnt(7)\n\tv_mfma_f32_32x32x16_bf16 v[0:15], v[200:203], v[116:119], v[0:15]\n\t"
          "v_mfma_f32_32x32x16_bf16 v[32:47], v[204:207], v[116:119], v[32:47]\n\ts_waitcnt vmcnt(6)\n\tv_mfma_f32_32x32x16_bf16 v[16:31], v[200:203], v[120:123], v[16:31]\n\tv_mfma_f32_32x32x16_bf16 v[48:63], v[204:207], v[120:123], v[48:63]\n\t"
          "s_waitcnt vmcnt(5)\n\tv_mfma_f32_32x32x16_bf16 v[0:15], v[208:211], v[124:127], v[0:15]\n\tv_mfma_f32_32x32x16_bf16 v[32:47], v[212:215], v[124:127], v[32:47]\n\ts_waitcnt vmcnt(4)\n\t"
          "v_mfma_f32_32x32x16_bf16 v[16:31], v[208:211], v[64:67], v[16:31]\n\tv_mfma_f32_32x32x16_bf16 v[48:63], v[212:215], v[64:67], v[48:63]\n\ts_waitcnt vmcnt(3)\n\tv_mfma_f32_32x32x16_bf16 v[0:15], v[216:219], v[68:71], v[0:15]\n\t"
          "v_mfma_f32_32x32x16_bf16 v[32:47], v[220:223], v[68:71], v[32:47]\n\ts_waitcnt vmcnt(2)\n\tv_mfma_f32_32x32x16_bf16 v[16:31], v[216:219], v[72:75], v[16:31]\n\tv_mfma_f32_32x32x16_bf16 v[48:63], v[220:223], v[72:75], v[48:63]\n\t"
          "s_waitcnt vmcnt(1)\n\tv_mfma_f32_32x32x16_bf16 v[0:15], v[224:227], v[76:79], v[0:15]\n\tv_mfma_f32_32x32x16_bf16 v[32:47], v[228:231], v[76:79], v[32:47]\n\ts_waitcnt vmcnt(0)\n\t"
          "v_mfma_f32_32x32x16_bf16 v[16:31], v[224:227], v[80:83], v[16:31]\n\tv_mfma_f32_32x32x16_bf16 v[48:63], v[228:231], v[80:83], v[48:63]\n\ts_branch HE%=\n\tH5%=:\n\t"
          "s_cmp_lg_u32 %[w], 5\n\ts_cbranch_scc1 H6%=\n\ts_add_u32 %[t0], %[j], 2\n\ts_and_b32 %[t0], %[t0], 7\n\t"
          "s_mul_i32 %[t0], %[t0], 28672\n\ts_add_u32 %[t0], %[t0], %[shb]\n\ts_add_u32 %[t0], %[t0], 26624\n\tbuffer_load_dwordx4 v[112:115], %[l16], %[desc], %[t0] offen offset:0 sc1\n\t"
          "buffer_load_dwordx4 v[116:119], %[l16], %[desc], %[t0] offen offset:1024 sc1\n\ts_add_u32 %[t0], %[j], 3\n\ts_and_b32 %[t0], %[t0], 7\n\ts_mul_i32 %[t0], %[t0], 28672\n\t"
          "s_add_u32 %[t0], %[t0], %[shb]\n\ts_add_u32 %[t0], %[t0], 16384\n\tbuffer_load_dwordx4 v[120:123], %[l16], %[desc], %[t0] offen offset:0 sc1\n\tbuffer_load_dwordx4 v[124:127], %[l16], %[desc], %[t0] offen offset:1024 sc1\n\t"
          "buffer_load_dwordx4 v[64:67], %[l16], %[desc], %[t0] offen offset:2048 sc1\n\tbuffer_load_dwordx4 v[68:71], %[l16], %[desc], %[t0] offen offset:3072 sc1\n\ts_add_u32 %[t0], %[t0], 4096\n\tbuffer_load_dwordx4 v[72:75], %[l16], %[desc], %[t0] offen offset:0 sc1\n\t"
          "buffer_load_dwordx4 v[76:79], %[l16], %[desc], %[t0] offen offset:1024 sc1\n\tbuffer_load_dwordx4 v[80:83], %[l16], %[desc], %[t0] offen offset:2048 sc1\n\tbuffer_load_dwordx4 v[96:99], %[l16], %[desc], %[t0] offen offset:3072 sc1\n\ts_add_u32 %[t0], %[t0], 4096\n\t"
          "buffer_load_dwordx4 v[100:103], %[l16], %[desc], %[t0] offen offset:0 sc1\n\tbuffer_load_dwordx4 v[104:107], %[l16], %[desc], %[t0] offen offset:1024 sc1\n\tbuffer_load_dwordx4 v[108:111], %[l16], %[desc], %[t0] offen offset:2048 sc1\n\ts_waitcnt vmcnt(12)\n\t"
          "v_mfma_f32_32x32x16_bf16 v[0:15], v[144:147], v[112:115], v[0:15]\n\tv_mfma_f32_32x32x16_bf16 v[32:47], v[148:151], v[112:115], v[32:47]\n\tbuffer_load_dwordx4 v[112:115], %[l16], %[desc], %[t0] offen offset:3072 sc1\n\ts_waitcnt vmcnt(12)\n\t"
          "v_mfma_f32_32x32x16_bf16 v[16:31], v[144:147], v[116:119], v[16:31]\n\tv_mfma_f32_32x32x16_bf16 v[48:63], v[148:151], v[116:119], v[48:63]\n\ts_add_u32 %[t0], %[j], 4\n\ts_and_b32 %[t0], %[t0], 7\n\t"
          "s_mul_i32 %[t0], %[t0], 28672\n\ts_add_u32 %[t0], %[t0], %[shb]\n\ts_add_u32 %[t0], %[t0], 16384\n\tbuffer_load_dwordx4 v[116:119], %[l16], %[desc], %[t0] offen offset:0 sc1\n\t"
          "s_waitcnt vmcnt(12)\n\tv_mfma_f32_32x32x16_bf16 v[0:15], v[152:155], v[120:123], v[0:15]\n\tv_mfma_f32_32x32x16_bf16 v[32:47], v[156:159], v[120:123], v[32:47]\n\tbuffer_load_dwordx4 v[120:123], %[l16], %[desc], %[t0] offen offset:1024 sc1\n\t"
          "s_waitcnt vmcnt(12)\n\tv_mfma_f32_32x32x16_bf16 v[16:31], v[152:155], v[124:127], v[16:31]\n\tv_mfma_f32_32x32x16_bf16 v[48:63], v[156:159], v[124:127], v[48:63]\n\tbuffer_load_dwordx4 v[124:127], %[l16], %[desc], %[t0] offen offset:2048 sc1\n\t"
          "s_waitcnt vmcnt(12)\n\tv_mfma_f32_32x32x16_bf16 v[0:15], v[160:163], v[64:67], v[0:15]\n\tv_mfma_f32_32x32x16_bf16 v[32:47], v[164:167], v[64:67], v[32:47]\n\tbuffer_load_dwordx4 v[64:67], %[l16], %[desc], %[t0] offen offset:3072 sc1\n\t"
          "s_add_u32 %[t0], %[t0], 4096\n\ts_waitcnt vmcnt(12)\n\tv_mfma_f32_32x32x16_bf16 v[16:31], v[160:163], v[68:71], v[16:31]\n\tv_mfma_f32_32x32x16_bf16 v[48:63], v[164:167], v[68:71], v[48:63]\n\t"
          "buffer_load_dwordx4 v[68:71], %[l16], %[desc], %[t0] offen offset:0 sc1\n\ts_waitcnt vmcnt(12)\n\tv_mfma_f32_32x32x16_bf16 v[0:15], v[168:171], v[72:75], v[0:15]\n\tv_mfma_f32_32x32x16_bf16 v[32:47], v[172:175], v[72:75], v[32:47]\n\t"
          "buffer_load_dwordx4 v[72:75], %[l16], %[desc], %[t0] offen offset:1024 sc1\n\ts_waitcnt vmcnt(12)\n\tv_mfma_f32_32x32x16_bf16 v[16:31], v[168:171], v[76:79], v[16:31]\n\tv_mfma_f32_32x32x16_bf16 v[48:63], v[172:175], v[76:79], v[48:63]\n\t"
          "buffer_load_dwordx4 v[76:79], %[l16], %[desc], %[t0] offen offset:2048 sc1\n\ts_waitcnt vmcnt(12)\n\tv_mfma_f32_32x32x16_bf16 v[0:15], v[176:179], v[80:83], v[0:15]\n\tv_mfma_f32_32x32x16_bf16 v[32:47], v[180:183], v[80:83], v[32:47]\n\t"
          "buffer_load_dwordx4 v[80:83], %[l16], %[desc], %[t0] offen offset:3072 sc1\n\ts_waitcnt vmcnt(12)\n\tv_mfma_f32_32x32x16_bf16 v[16:31], v[176:179], v[96:99], v[16:31]\n\tv_mfma_f32_32x32x16_bf16 v[48:63], v[180:183], v[96:99], v[48:63]\n\t"
          "s_waitcnt vmcnt(11)\n\tv_mfma_f32_32x32x16_bf16 v[0:15], v[184:187], v[100:103], v[0:15]\n\tv_mfma_f32_32x32x16_bf16 v[32:47], v[188:191], v[100:103], v[32:47]\n\ts_waitcnt vmcnt(10)\n\t"
          "v_mfma_f32_32x32x16_bf16 v[16:31], v[184:187], v[104:107], v[16:31]\n\tv_mfma_f32_32x32x16_bf16 v[48:63], v[188:191], v[104:107], v[48:63]\n\ts_waitcnt vmcnt(9)\n\tv_mfma_f32_32x32x16_bf16 v[0:15], v[192:195], v[108:111], v[0:15]\n\t"
          "v_mfma_f32_32x32x16_bf16 v[32:47], v[196:199], v[108:111], v[32:47]\n\ts_waitcnt vmcnt(8)\n\tv_mfma_f32_32x32x16_bf16 v[16:31], v[192:195], v[112:115], v[16:31]\n\tv_mfma_f32_32x32x16_bf16 v[48:63], v[196:199], v[112:115], v[48:63]\n\t"
          "s_waitcnt vmcnt(7)\n\tv_mfma_f32_32x32x16_bf16 v[0:15], v[200:203], v[116:119], v[0:15]\n\tv_mfma_f32_32x32x16_bf16 v[32:47], v[204:207], v[116:119], v[32:47]\n\ts_waitcnt vmcnt(6)\n\t"
          "v_mfma_f32_32x32x16_bf16 v[16:31], v[200:203], v[120:123], v[16:31]\n\tv_mfma_f32_32x32x16_bf16 v[48:63], v[204:207], v[120:123], v[48:63]\n\ts_waitcnt vmcnt(5)\n\tv_mfma_f32_32x32x16_bf16 v[0:15], v[208:211], v[124:127], v[0:15]\n\t"
          "v_mfma_f32_32x32x16_bf16 v[32:47], v[212:215], v[124:127], v[32:47]\n\ts_waitcnt vmcnt(4)\n\tv_mfma_f32_32x32x16_bf16 v[16:31], v[208:211], v[64:67], v[16:31]\n\tv_mfma_f32_32x32x16_bf16 v[48:63], v[212:215], v[64:67], v[48:63]\n\t"
          "s_waitcnt vmcnt(3)\n\tv_mfma_f32_32x32x16_bf16 v[0:15], v[216:219], v[68:71], v[0:15]\n\tv_mfma_f32_32x32x16_bf16 v[32:47], v[220:223], v[68:71], v[32:47]\n\ts_waitcnt vmcnt(2)\n\t"
          "v_mfma_f32_32x32x16_bf16 v[16:31], v[216:219], v[72:75], v[16:31]\n\tv_mfma_f32_32x32x16_bf16 v[48:63], v[220:223], v[72:75], v[48:63]\n\ts_waitcnt vmcnt(1)\n\tv_mfma_f32_32x32x16_bf16 v[0:15], v[224:227], v[76:79], v[0:15]\n\t"
          "v_mfma_f32_32x32x16_bf16 v[32:47], v[228:231], v[76:79], v[32:47]\n\ts_waitcnt vmcnt(0)\n\tv_mfma_f32_32x32x16_bf16 v[16:31], v[224:227], v[80:83], v[16:31]\n\tv_mfma_f32_32x32x16_bf16 v[48:63], v[228:231], v[80:83], v[48:63]\n\t"
          "s_branch HE%=\n\tH6%=:\n\ts_cmp_lg_u32 %[w], 6\n\ts_cbranch_scc1 H7%=\n\t"
          "s_add_u32 %[t0], %[j], 4\n\ts_and_b32 %[t0], %[t0], 7\n\ts_mul_i32 %[t0], %[t0], 28672\n\ts_add_u32 %[t0], %[t0], %[shb]\n\t"
          "s_add_u32 %[t0], %[t0], 24576\n\tbuffer_load_dwordx4 v[112:115], %[l16], %[desc], %[t0] offen offset:0 sc1\n\tbuffer_load_dwordx4 v[116:119], %[l16], %[desc], %[t0] offen offset:1024 sc1\n\tbuffer_load_dwordx4 v[120:123], %[l16], %[desc], %[t0] offen offset:2048 sc1\n\t"
          "buffer_load_dwordx4 v[124:127], %[l16], %[desc], %[t0] offen offset:3072 sc1\n\ts_add_u32 %[t0], %[j], 5\n\ts_and_b32 %[t0], %[t0], 7\n\ts_mul_i32 %[t0], %[t0], 28672\n\t"
          "s_add_u32 %[t0], %[t0], %[shb]\n\ts_add_u32 %[t0], %[t0], 16384\n\tbuffer_load_dwordx4 v[64:67], %[l16], %[desc], %[t0] offen offset:0 sc1\n\tbuffer_load_dwordx4 v[68:71], %[l16], %[desc], %[t0] offen offset:1024 sc1\n\t"
          "buffer_load_dwordx4 v[72:75], %[l16], %[desc], %[t0] offen offset:2048 sc1\n\tbuffer_load_dwordx4 v[76:79], %[l16], %[desc], %[t0] offen offset:3072 sc1\n\ts_add_u32 %[t0], %[t0], 4096\n\tbuffer_load_dwordx4 v[80:83], %[l16], %[desc], %[t0] offen offset:0 sc1\n\t"
          "buffer_load_dwordx4 v[96:99], %[l16], %[desc], %[t0] offen offset:1024 sc1\n\tbuffer_load_dwordx4 v[100:103], %[l16], %[desc], %[t0] offen offset:2048 sc1\n\tbuffer_load_dwordx4 v[104:107], %[l16], %[desc], %[t0] offen offset:3072 sc1\n\ts_add_u32 %[t0], %[t0], 4096\n\t"
          "buffer_load_dwordx4 v[108:111], %[l16], %[desc], %[t0] offen offset:0 sc1\n\ts_waitcnt vmcnt(12)\n\tv_mfma_f32_32x32x16_bf16 v[0:15], v[128:131], v[112:115], 0\n\tv_mfma_f32_32x32x16_bf16 v[32:47], v[132:135], v[112:115], 0\n\t"
          "buffer_load_dwordx4 v[112:115], %[l16], %[desc], %[t0] offen offset:1024 sc1\n\ts_waitcnt vmcnt(12)\n\tv_mfma_f32_32x32x16_bf16 v[16:31], v[128:131], v[116:119], 0\n\tv_mfma_f32_32x32x16_bf16 v[48:63], v[132:135], v[116:119], 0\n\t"
          "buffer_load_dwordx4 v[116:119], %[l16], %[desc], %[t0] offen offset:2048 sc1\n\ts_waitcnt vmcnt(12)\n\tv_mfma_f32_32x32x16_bf16 v[0:15], v[136:139], v[120:123], v[0:15]\n\tv_mfma_f32_32x32x16_bf16 v[32:47], v[140:143], v[120:123], v[32:47]\n\t"
          "buffer_load_dwordx4 v[120:123], %[l16], %[desc], %[t0] offen offset:3072 sc1\n\ts_waitcnt vmcnt(12)\n\tv_mfma_f32_32x32x16_bf16 v[16:31], v[136:139], v[124:127], v[16:31]\n\tv_mfma_f32_32x32x16_bf16 v[48:63], v[140:143], v[124:127], v[48:63]\n\t"
          "s_add_u32 %[t0], %[j], 6\n\ts_and_b32 %[t0], %[t0], 7\n\ts_mul_i32 %[t0], %[t0], 28672\n\ts_add_u32 %[t0], %[t0], %[shb]\n\t"
          "s_add_u32 %[t0], %[t0], 16384\n\tbuffer_load_dwordx4 v[124:127], %[l16], %[desc], %[t0] offen offset:0 sc1\n\ts_waitcnt vmcnt(12)\n\tv_mfma_f32_32x32x16_bf16 v[0:15], v[144:147], v[64:67], v[0:15]\n\t"
          "v_mfma_f32_32x32x16_bf16 v[32:47], v[148:151], v[64:67], v[32:47]\n\tbuffer_load_dwordx4 v[64:67], %[l16], %[desc], %[t0] offen offset:1024 sc1\n\ts_waitcnt vmcnt(12)\n\tv_mfma_f32_32x32x16_bf16 v[16:31], v[144:147], v[68:71], v[16:31]\n\t"
          "v_mfma_f32_32x32x16_bf16 v[48:63], v[148:151], v[68:71], v[48:63]\n\tbuffer_load_dwordx4 v[68:71], %[l16], %[desc], %[t0] offen offset:2048 sc1\n\ts_waitcnt vmcnt(12)\n\tv_mfma_f32_32x32x16_bf16 v[0:15], v[152:155], v[72:75], v[0:15]\n\t"
          "v_mfma_f32_32x32x16_bf16 v[32:47], v[156:159], v[72:75], v[32:47]\n\tbuffer_load_dwordx4 v[72:75], %[l16], %[desc], %[t0] offen offset:3072 sc1\n\ts_add_u32 %[t0], %[t0], 4096\n\ts_waitcnt vmcnt(12)\n\t"
          "v_mfma_f32_32x32x16_bf16 v[16:31], v[152:155], v[76:79], v[16:31]\n\tv_mfma_f32_32x32x16_bf16 v[48:63], v[156:159], v[76:79], v[48:63]\n\tbuffer_load_dwordx4 v[76:79], %[l16], %[desc], %[t0] offen offset:0 sc1\n\ts_waitcnt vmcnt(12)\n\t"
          "v_mfma_f32_32x32x16_bf16 v[0:15], v[160:163], v[80:83], v[0:15]\n\tv_mfma_f32_32x32x16_bf16 v[32:47], v[164:167], v[80:83], v[32:47]\n\tbuffer_load_dwordx4 v[80:83], %[l16], %[desc], %[t0] offen offset:1024 sc1\n\ts_waitcnt vmcnt(12)\n\t"
          "v_mfma_f32_32x32x16_bf16 v[16:31], v[160:163], v[96:99], v[16:31]\n\tv_mfma_f32_32x32x16_bf16 v[48:63], v[164:167], v[96:99], v[48:63]\n\ts_waitcnt vmcnt(11)\n\tv_mfma_f32_32x32x16_bf16 v[0:15], v[168:171], v[100:103], v[0:15]\n\t"
          "v_mfma_f32_32x32x16_bf16 v[32:47], v[172:175], v[100:103], v[32:47]\n\ts_waitcnt vmcnt(10)\n\tv_mfma_f32_32x32x16_bf16 v[16:31], v[168:171], v[104:107], v[16:31]\n\tv_mfma_f32_32x32x16_bf16 v[48:63], v[172:175], v[104:107], v[48:63]\n\t"
          "s_waitcnt vmcnt(9)\n\tv_mfma_f32_32x32x16_bf16 v[0:15], v[176:179], v[108:111], v[0:15]\n\tv_mfma_f32_32x32x16_bf16 v[32:47], v[180:183], v[108:111], v[32:47]\n\ts_waitcnt vmcnt(8)\n\t"
          "v_mfma_f32_32x32x16_bf16 v[16:31], v[176:179], v[112:115], v[16:31]\n\tv_mfma_f32_32x32x16_bf16 v[48:63], v[180:183], v[112:115], v[48:63]\n\ts_waitcnt vmcnt(7)\n\tv_mfma_f32_32x32x16_bf16 v[0:15], v[184:187], v[116:119], v[0:15]\n\t"
          "v_mfma_f32_32x32x16_bf16 v[32:47], v[188:191], v[116:119], v[32:47]\n\ts_waitcnt vmcnt(6)\n\tv_mfma_f32_32x32x16_bf16 v[16:31], v[184:187], v[120:123], v[16:31]\n\tv_mfma_f32_32x32x16_bf16 v[48:63], v[188:191], v[120:123], v[48:63]\n\t"
          "s_waitcnt vmcnt(5)\n\tv_mfma_f32_32x32x16_bf16 v[0:15], v[192:195], v[124:127], v[0:15]\n\tv_mfma_f32_32x32x16_bf16 v[32:47], v[196:199], v[124:127], v[32:47]\n\ts_waitcnt vmcnt(4)\n\t"
          "v_mfma_f32_32x32x16_bf16 v[16:31], v[192:195], v[64:67], v[16:31]\n\tv_mfma_f32_32x32x16_bf16 v[48:63], v[196:199], v[64:67], v[48:63]\n\ts_waitcnt vmcnt(3)\n\tv_mfma_f32_32x32x16_bf16 v[0:15], v[200:203], v[68:71], v[0:15]\n\t"
          "v_mfma_f32_32x32x16_bf16 v[32:47], v[204:207], v[68:71], v[32:47]\n\ts_waitcnt vmcnt(2)\n\tv_mfma_f32_32x32x16_bf16 v[16:31], v[200:203], v[72:75], v[16:31]\n\tv_mfma_f32_32x32x16_bf16 v[48:63], v[204:207], v[72:75], v[48:63]\n\t"
          "s_waitcnt vmcnt(1)\n\tv_mfma_f32_32x32x16_bf16 v[0:15], v[208:211], v[76:79], v[0:15]\n\tv_mfma_f32_32x32x16_bf16 v[32:47], v[212:215], v[76:79], v[32:47]\n\ts_waitcnt vmcnt(0)\n\t"
          "v_mfma_f32_32x32x16_bf16 v[16:31], v[208:211], v[80:83], v[16:31]\n\tv_mfma_f32_32x32x16_bf16 v[48:63], v[212:215], v[80:83], v[48:63]\n\ts_branch HE%=\n\tH7%=:\n\t"
          "s_add_u32 %[t0], %[j], 6\n\ts_and_b32 %[t0], %[t0], 7\n\ts_mul_i32 %[t0], %[t0], 28672\n\ts_add_u32 %[t0], %[t0], %[shb]\n\t"
          "s_add_u32 %[t0], %[t0], 22528\n\tbuffer_load_dwordx4 v[112:115], %[l16], %[desc], %[t0] offen offset:0 sc1\n\tbuffer_load_dwordx4 v[116:119], %[l16], %[desc], %[t0] offen offset:1024 sc1\n\tbuffer_load_dwordx4 v[120:123], %[l16], %[desc], %[t0] offen offset:2048 sc1\n\t"
          "buffer_load_dwordx4 v[124:127], %[l16], %[desc], %[t0] offen offset:3072 sc1\n\ts_add_u32 %[t0], %[t0], 4096\n\tbuffer_load_dwordx4 v[64:67], %[l16], %[desc], %[t0] offen offset:0 sc1\n\tbuffer_load_dwordx4 v[68:71], %[l16], %[desc], %[t0] offen offset:1024 sc1\n\t"
          "s_add_u32 %[t0], %[j], 7\n\ts_and_b32 %[t0], %[t0], 7\n\ts_mul_i32 %[t0], %[t0], 28672\n\ts_add_u32 %[t0], %[t0], %[shb]\n\t"
          "s_add_u32 %[t0], %[t0], 16384\n\tbuffer_load_dwordx4 v[72:75], %[l16], %[desc], %[t0] offen offset:0 sc1\n\tbuffer_load_dwordx4 v[76:79], %[l16], %[desc], %[t0] offen offset:1024 sc1\n\tbuffer_load_dwordx4 v[80:83], %[l16], %[desc], %[t0] offen offset:2048 sc1\n\t"
          "buffer_load_dwordx4 v[96:99], %[l16], %[desc], %[t0] offen offset:3072 sc1\n\ts_add_u32 %[t0], %[t0], 4096\n\tbuffer_load_dwordx4 v[100:103], %[l16], %[desc], %[t0] offen offset:0 sc1\n\tbuffer_load_dwordx4 v[104:107], %[l16], %[desc], %[t0] offen offset:1024 sc1\n\t"
          "buffer_load_dwordx4 v[108:111], %[l16], %[desc], %[t0] offen offset:2048 sc1\n\ts_waitcnt vmcnt(12)\n\tv_mfma_f32_32x32x16_bf16 v[0:15], v[128:131], v[112:115], 0\n\tv_mfma_f32_32x32x16_bf16 v[32:47], v[132:135], v[112:115], 0\n\t"
          "buffer_load_dwordx4 v[112:115], %[l16], %[desc], %[t0] offen offset:3072 sc1\n\ts_add_u32 %[t0], %[t0], 4096\n\ts_waitcnt vmcnt(12)\n\tv_mfma_f32_32x32x16_bf16 v[16:31], v[128:131], v[116:119], 0\n\t"
          "v_mfma_f32_32x32x16_bf16 v[48:63], v[132:135], v[116:119], 0\n\tbuffer_load_dwordx4 v[116:119], %[l16], %[desc], %[t0] offen offset:0 sc1\n\ts_waitcnt vmcnt(12)\n\tv_mfma_f32_32x32x16_bf16 v[0:15], v[136:139], v[120:123], v[0:15]\n\t"
          "v_mfma_f32_32x32x16_bf16 v[32:47], v[140:143], v[120:123], v[32:47]\n\tbuffer_load_dwordx4 v[120:123], %[l16], %[desc], %[t0] offen offset:1024 sc1\n\ts_waitcnt vmcnt(12)\n\tv_mfma_f32_32x32x16_bf16 v[16:31], v[136:139], v[124:127], v[16:31]\n\t"
          "v_mfma_f32_32x32x16_bf16 v[48:63], v[140:143], v[124:127], v[48:63]\n\tbuffer_load_dwordx4 v[124:127], %[l16], %[desc], %[t0] offen offset:2048 sc1\n\ts_waitcnt vmcnt(12)\n\tv_mfma_f32_32x32x16_bf16 v[0:15], v[144:147], v[64:67], v[0:15]\n\t"
          "v_mfma_f32_32x32x16_bf16 v[32:47], v[148:151], v[64:67], v[32:47]\n\tbuffer_load_dwordx4 v[64:67], %[l16], %[desc], %[t0] offen offset:3072 sc1\n\ts_waitcnt vmcnt(12)\n\tv_mfma_f32_32x32x16_bf16 v[16:31], v[144:147], v[68:71], v[16:31]\n\t"
          "v_mfma_f32_32x32x16_bf16 v[48:63], v[148:151], v[68:71], v[48:63]\n\ts_add_u32 %[t0], %[j], 0\n\ts_and_b32 %[t0], %[t0], 7\n\ts_mul_i32 %[t0], %[t0], 28672\n\t"
          "s_add_u32 %[t0], %[t0], %[shb]\n\ts_add_u32 %[t0], %[t0], 24576\n\tbuffer_load_dwordx4 v[68:71], %[l16], %[desc], %[t0] offen offset:0 sc1\n\ts_waitcnt vmcnt(12)\n\t"
          "v_mfma_f32_32x32x16_bf16 v[0:15], v[152:155], v[72:75], v[0:15]\n\tv_mfma_f32_32x32x16_bf16 v[32:47], v[156:159], v[72:75], v[32:47]\n\tbuffer_load_dwordx4 v[72:75], %[l16], %[desc], %[t0] offen offset:1024 sc1\n\ts_waitcnt vmcnt(12)\n\t"
          "v_mfma_f32_32x32x16_bf16 v[16:31], v[152:155], v[76:79], v[16:31]\n\tv_mfma_f32_32x32x16_bf16 v[48:63], v[156:159], v[76:79], v[48:63]\n\tbuffer_load_dwordx4 v[76:79], %[l16], %[desc], %[t0] offen offset:2048 sc1\n\ts_waitcnt vmcnt(12)\n\t"
          "v_mfma_f32_32x32x16_bf16 v[0:15], v[160:163], v[80:83], v[0:15]\n\tv_mfma_f32_32x32x16_bf16 v[32:47], v[164:167], v[80:83], v[32:47]\n\tbuffer_load_dwordx4 v[80:83], %[l16], %[desc], %[t0] offen offset:3072 sc1\n\ts_waitcnt vmcnt(12)\n\t"
          "v_mfma_f32_32x32x16_bf16 v[16:31], v[160:163], v[96:99], v[16:31]\n\tv_mfma_f32_32x32x16_bf16 v[48:63], v[164:167], v[96:99], v[48:63]\n\ts_waitcnt vmcnt(11)\n\tv_mfma_f32_32x32x16_bf16 v[0:15], v[168:171], v[100:103], v[0:15]\n\t"
          "v_mfma_f32_32x32x16_bf16 v[32:47], v[172:175], v[100:103], v[32:47]\n\ts_waitcnt vmcnt(10)\n\tv_mfma_f32_32x32x16_bf16 v[16:31], v[168:171], v[104:107], v[16:31]\n\tv_mfma_f32_32x32x16_bf16 v[48:63], v[172:175], v[104:107], v[48:63]\n\t"
          "s_waitcnt vmcnt(9)\n\tv_mfma_f32_32x32x16_bf16 v[0:15], v[176:179], v[108:111], v[0:15]\n\tv_mfma_f32_32x32x16_bf16 v[32:47], v[180:183], v[108:111], v[32:47]\n\ts_waitcnt vmcnt(8)\n\t"
          "v_mfma_f32_32x32x16_bf16 v[16:31], v[176:179], v[112:115], v[16:31]\n\tv_mfma_f32_32x32x16_bf16 v[48:63], v[180:183], v[112:115], v[48:63]\n\ts_waitcnt vmcnt(7)\n\tv_mfma_f32_32x32x16_bf16 v[0:15], v[184:187], v[116:119], v[0:15]\n\t"
          "v_mfma_f32_32x32x16_bf16 v[32:47], v[188:191], v[116:119], v[32:47]\n\ts_waitcnt vmcnt(6)\n\tv_mfma_f32_32x32x16_bf16 v[16:31], v[184:187], v[120:123], v[16:31]\n\tv_mfma_f32_32x32x16_bf16 v[48:63], v[188:191], v[120:123], v[48:63]\n\t"
          "s_waitcnt vmcnt(5)\n\tv_mfma_f32_32x32x16_bf16 v[0:15], v[192:195], v[124:127], v[0:15]\n\tv_mfma_f32_32x32x16_bf16 v[32:47], v[196:199], v[124:127], v[32:47]\n\ts_waitcnt vmcnt(4)\n\t"
          "v_mfma_f32_32x32x16_bf16 v[16:31], v[192:195], v[64:67], v[16:31]\n\tv_mfma_f32_32x32x16_bf16 v[48:63], v[196:199], v[64:67], v[48:63]\n\ts_waitcnt vmcnt(3)\n\tv_mfma_f32_32x32x16_bf16 v[0:15], v[200:203], v[68:71], v[0:15]\n\t"
          "v_mfma_f32_32x32x16_bf16 v[32:47], v[204:207], v[68:71], v[32:47]\n\ts_waitcnt vmcnt(2)\n\tv_mfma_f32_32x32x16_bf16 v[16:31], v[200:203], v[72:75], v[16:31]\n\tv_mfma_f32_32x32x16_bf16 v[48:63], v[204:207], v[72:75], v[48:63]\n\t"
          "s_waitcnt vmcnt(1)\n\tv_mfma_f32_32x32x16_bf16 v[0:15], v[208:211], v[76:79], v[0:15]\n\tv_mfma_f32_32x32x16_bf16 v[32:47], v[212:215], v[76:79], v[32:47]\n\ts_waitcnt vmcnt(0)\n\t"
          "v_mfma_f32_32x32x16_bf16 v[16:31], v[208:211], v[80:83], v[16:31]\n\tv_mfma_f32_32x32x16_bf16 v[48:63], v[212:215], v[80:83], v[48:63]\n\tHE%=:\n\tds_read_b128 v[96:99], %[vxr]\n\t"
          "ds_read_b128 v[108:111], %[vtab] offset:640\n\tds_read_b128 v[112:115], %[vtab] offset:672\n\tv_mov_b64 v[116:117], %[tag]\n\tv_mov_b64 v[118:119], %[tag]\n\t"
          "v_add_u32 v120, 0x10000, %[vwr]\n\ts_nop 5\n\tds_write_b128 %[vwr], v[0:3] offset:0\n\tds_write_b128 %[vwr], v[4:7] offset:1024\n\t"
          "ds_write_b128 %[vwr], v[16:19] offset:16384\n\tds_write_b128 %[vwr], v[20:23] offset:17408\n\tds_write_b128 %[vwr], v[8:11] offset:32768\n\tds_write_b128 %[vwr], v[12:15] offset:33792\n\t"
          "ds_write_b128 %[vwr], v[24:27] offset:49152\n\tds_write_b128 %[vwr], v[28:31] offset:50176\n\ts_waitcnt lgkmcnt(6)\n\tds_write_b128 v120, v[32:35] offset:0\n\t"
          "ds_write_b128 v120, v[36:39] offset:1024\n\tds_write_b128 v120, v[48:51] offset:16384\n\tds_write_b128 v120, v[52:55] offset:17408\n\tds_write_b128 v120, v[40:43] offset:32768\n\t"
          "ds_write_b128 v120, v[44:47] offset:33792\n\tds_write_b128 v120, v[56:59] offset:49152\n\tds_write_b128 v120, v[60:63] offset:50176\n\ts_waitcnt lgkmcnt(0)\n\t"
          "s_barrier\n\ts_cmp_ge_u32 %[w], 4\n\ts_cbranch_scc1 HF%=\n\ts_cmp_lg_u32 %[lay], 0\n\t"
          "s_cbranch_scc1 HF%=\n\tbuffer_load_dwordx4 v[160:163], %[l16], %[descq], %[sq] offen offset:0 sc1\n\tbuffer_load_dwordx4 v[164:167], %[l16], %[descq], %[sq] offen offset:1024 sc1\n\tbuffer_load_dwordx4 v[168:171], %[l16], %[descq], %[sq] offen offset:2048 sc1\n\t"
          "buffer_load_dwordx4 v[172:175], %[l16], %[descq], %[sq] offen offset:3072 sc1\n\ts_add_u32 %[sq], %[sq], 4096\n\tbuffer_load_dwordx4 v[176:179], %[l16], %[descq], %[sq] offen offset:0 sc1\n\tbuffer_load_dwordx4 v[180:183], %[l16], %[descq], %[sq] offen offset:1024 sc1\n\t"
          "buffer_load_dwordx4 v[184:187], %[l16], %[descq], %[sq] offen offset:2048 sc1\n\tbuffer_load_dwordx4 v[188:191], %[l16], %[descq], %[sq] offen offset:3072 sc1\n\tHF%=:\n\tds_read_b128 v[0:3], %[vrd] offset:0\n\t"
          "ds_read_b128 v[4:7], %[vrd] offset:1024\n\tds_read_b128 v[8:11], %[vrd] offset:2048\n\tds_read_b128 v[12:15], %[vrd] offset:3072\n\tds_read_b128 v[16:19], %[vrd] offset:4096\n\t"
          "ds_read_b128 v[20:23], %[vrd] offset:5120\n\tds_read_b128 v[24:27], %[vrd] offset:6144\n\tds_read_b128 v[28:31], %[vrd] offset:7168\n\tds_read_b128 v[32:35], %[vrd] offset:8192\n\t"
          "ds_read_b128 v[36:39], %[vrd] offset:9216\n\tds_read_b128 v[40:43], %[vrd] offset:10240\n\tds_read_b128 v[44:47], %[vrd] offset:11264\n\tds_read_b128 v[48:51], %[vrd] offset:12288\n\t"
          "ds_read_b128 v[52:55], %[vrd] offset:13312\n\tds_read_b128 v[56:59], %[vrd] offset:14336\n\ts_waitcnt lgkmcnt(13)\n\tds_read_b128 v[60:63], %[vrd] offset:15360\n\t"
          "v_lshlrev_b32 v100, 16, v96\n\tv_and_b32 v101, 0xffff0000, v96\n\tv_lshlrev_b32 v102, 16, v97\n\tv_and_b32 v103, 0xffff0000, v97\n\t"
          "v_lshlrev_b32 v104, 16, v98\n\tv_and_b32 v105, 0xffff0000, v98\n\tv_lshlrev_b32 v106, 16, v99\n\tv_and_b32 v107, 0xffff0000, v99\n\t"
          "v_pk_add_f32 v[100:101], v[100:101], v[108:109]\n\tv_pk_add_f32 v[102:103], v[102:103], v[110:111]\n\tv_pk_add_f32 v[104:105], v[104:105], v[112:113]\n\tv_pk_add_f32 v[106:107], v[106:107], v[114:115]\n\t"
          "s_waitcnt lgkmcnt(12)\n\tv_pk_add_f32 v[0:1], v[0:1], v[8:9]\n\tv_pk_add_f32 v[2:3], v[2:3], v[10:11]\n\tv_pk_add_f32 v[4:5], v[4:5], v[12:13]\n\t"
          "v_pk_add_f32 v[6:7], v[6:7], v[14:15]\n\ts_waitcnt lgkmcnt(8)\n\tv_pk_add_f32 v[16:17], v[16:17], v[24:25]\n\tv_pk_add_f32 v[18:19], v[18:19], v[26:27]\n\t"
          "v_pk_add_f32 v[20:21], v[20:21], v[28:29]\n\tv_pk_add_f32 v[22:23], v[22:23], v[30:31]\n\tv_pk_add_f32 v[0:1], v[0:1], v[16:17]\n\tv_pk_add_f32 v[2:3], v[2:3], v[18:19]\n\t"
          "v_pk_add_f32 v[4:5], v[4:5], v[20:21]\n\tv_pk_add_f32 v[6:7], v[6:7], v[22:23]\n\ts_waitcnt lgkmcnt(4)\n\tv_pk_add_f32 v[32:33], v[32:33], v[40:41]\n\t"
          "v_pk_add_f32 v[34:35], v[34:35], v[42:43]\n\tv_pk_add_f32 v[36:37], v[36:37], v[44:45]\n\tv_pk_add_f32 v[38:39], v[38:39], v[46:47]\n\tv_pk_add_f32 v[0:1], v[0:1], v[32:33]\n\t"
          "v_pk_add_f32 v[2:3], v[2:3], v[34:35]\n\tv_pk_add_f32 v[4:5], v[4:5], v[36:37]\n\tv_pk_add_f32 v[6:7], v[6:7], v[38:39]\n\ts_waitcnt lgkmcnt(0)\n\t"
          "v_pk_add_f32 v[48:49], v[48:49], v[56:57]\n\tv_pk_add_f32 v[50:51], v[50:51], v[58:59]\n\tv_pk_add_f32 v[52:53], v[52:53], v[60:61]\n\tv_pk_add_f32 v[54:55], v[54:55], v[62:63]\n\t"
          "v_pk_add_f32 v[0:1], v[0:1], v[48:49]\n\tv_pk_add_f32 v[2:3], v[2:3], v[50:51]\n\tv_pk_add_f32 v[4:5], v[4:5], v[52:53]\n\tv_pk_add_f32 v[6:7], v[6:7], v[54:55]\n\t"
          "v_pk_add_f32 v[100:101], v[100:101], v[0:1]\n\tv_pk_add_f32 v[102:103], v[102:103], v[2:3]\n\tv_pk_add_f32 v[104:105], v[104:105], v[4:5]\n\tv_pk_add_f32 v[106:107], v[106:107], v[6:7]\n\t"
          "v_cvt_pk_bf16_f32 v96, v100, v101\n\tv_cvt_pk_bf16_f32 v97, v102, v103\n\tv_cvt_pk_bf16_f32 v98, v104, v105\n\tv_cvt_pk_bf16_f32 v99, v106, v107\n\t"
          "s_cmp_lg_u32 %[lay], 0\n\ts_cbranch_scc1 HL%=\n\tbuffer_store_dwordx4 v[96:99], %[l16], %[desc], %[sxo] offen sc1\n\tv_mov_b32 v100, 0\n\t"
          "v_mov_b32 v101, 0\n\tv_mov_b32 v102, 0\n\tv_mov_b32 v103, 0\n\tv_dot2c_f32_bf16 v100, 0x3f803f80, v96\n\t"
          "v_dot2c_f32_bf16 v101, v96, v96\n\tv_dot2c_f32_bf16 v102, 0x3f803f80, v97\n\tv_dot2c_f32_bf16 v103, v97, v97\n\tv_dot2c_f32_bf16 v100, 0x3f803f80, v98\n\t"
          "v_dot2c_f32_bf16 v101, v98, v98\n\tv_dot2c_f32_bf16 v102, 0x3f803f80, v99\n\tv_dot2c_f32_bf16 v103, v99, v99\n\tds_write_b128 %[vxr], v[96:99]\n\t"
          "v_lshrrev_b32 v104, 1, %[l16]\n\ts_nop 0\n\tv_pk_add_f32 v[100:101], v[100:101], v[102:103]\n\tv_mov_b64 v[102:103], v[100:101]\n\t"
          "s_nop 1\n\tv_permlane32_swap_b32 v100, v102\n\tv_permlane32_swap_b32 v101, v103\n\tv_pk_add_f32 v[100:101], v[100:101], v[102:103]\n\t"
          "s_mov_b32 exec_hi, 0\n\tbuffer_store_dwordx2 v[100:101], v104, %[desc], %[sso] offen sc1\n\ts_mov_b32 exec_hi, -1\n\tv_lshlrev_b32 v64, 3, %[l16]\n\t"
          "s_mov_b32 m0, %[mdu]\n\ts_waitcnt vmcnt(0) lgkmcnt(0)\n\ts_barrier\n\ts_cmp_lg_u32 %[w], 0\n\t"
          "s_cbranch_scc1 HQ%=\n\ts_mov_b64 exec, 0xff\n\tbuffer_store_dwordx4 v[116:119], %[l16], %[desc], %[sf] offen sc1\n\ts_mov_b64 exec, -1\n\t"
          "s_nop 1\n\tHQ%=:\n\ts_nop 7\n\ts_mov_b64 exec, 0xff\n\t"
          "buffer_load_dword v64, %[desc], %[sxo] offen sc1 lds\n\ts_mov_b64 exec, 0x3\n\tbuffer_load_dword v64, %[desc], %[sso] offen sc1 lds\n\ts_mov_b64 exec, -1\n\t"
          "s_cmp_ge_u32 %[w], 4\n\ts_cbranch_scc1 HZ%=\n\ts_add_u32 %[sq], %[sq], 4096\n\tbuffer_load_dwordx4 v[192:195], %[l16], %[desc], %[sq] offen offset:0 sc1\n\t"
          "buffer_load_dwordx4 v[196:199], %[l16], %[desc], %[sq] offen offset:1024 sc1\n\tbuffer_load_dwordx4 v[200:203], %[l16], %[desc], %[sq] offen offset:2048 sc1\n\tbuffer_load_dwordx4 v[204:207], %[l16], %[desc], %[sq] offen offset:3072 sc1\n\ts_add_u32 %[sq], %[sq], 4096\n\t"
          "buffer_load_dwordx4 v[208:211], %[l16], %[desc], %[sq] offen offset:0 sc1\n\tbuffer_load_dwordx4 v[212:215], %[l16], %[desc], %[sq] offen offset:1024 sc1\n\tbuffer_load_dwordx4 v[216:219], %[l16], %[desc], %[sq] offen offset:2048 sc1\n\tbuffer_load_dwordx4 v[220:223], %[l16], %[desc], %[sq] offen offset:3072 sc1\n\t"
          "s_branch HX%=\n\tHZ%=:\n\ts_add_u32 %[sq], %[sq], 6144\n\tbuffer_load_dwordx4 v[128:131], %[l16], %[desc], %[sq] offen offset:0 sc1\n\t"
          "buffer_load_dwordx4 v[132:135], %[l16], %[desc], %[sq] offen offset:1024 sc1\n\tbuffer_load_dwordx4 v[136:139], %[l16], %[desc], %[sq] offen offset:2048 sc1\n\tbuffer_load_dwordx4 v[140:143], %[l16], %[desc], %[sq] offen offset:3072 sc1\n\ts_add_u32 %[sq], %[sq], 4096\n\t"
          "buffer_load_dwordx4 v[144:147], %[l16], %[desc], %[sq] offen offset:0 sc1\n\tbuffer_load_dwordx4 v[148:151], %[l16], %[desc], %[sq] offen offset:1024 sc1\n\tbuffer_load_dwordx4 v[152:155], %[l16], %[desc], %[sq] offen offset:2048 sc1\n\tbuffer_load_dwordx4 v[156:159], %[l16], %[desc], %[sq] offen offset:3072 sc1\n\t"
          "s_branch HX%=\n\tHL%=:\n\ts_nop 0\n\tv_permlane32_swap_b32 v96, v98\n\t"
          "v_permlane32_swap_b32 v97, v99\n\tbuffer_store_dwordx4 v[96:99], %[vo], %[desco], 0 offen\n\ts_nop 1\n\tHX%=:\n\t"
          : "+{v[96:111]}"(hp), [sq] "+s"(sq), [bud] "+s"(bud), [t0] "=&s"(t0)
          : [l16] "v"(l16), [vf] "v"(flag_off(SL_H + sl, c * 8 + (lane & 7))), [vxr] "v"(lds0 + L_XRES + w * 1024 + l16), [vtab] "v"(ltab + h * 16),
            [vwr] "v"(lds0 + w * 2048 + l16), [vrd] "v"(lds0 + w * 16384 + l16),
            [vo] "v"(((64 * c + 32 * (w & 1) + tok) * DM + 64 * j + 16 * (w >> 1) + 8 * h) * 2),
            [desc] "s"(desc), [descq] "s"(mkdesc(ws, layer == 0 ? WS_BYTES : 0u)), [desco] "s"(mkdesc(out, 2048 * DM * 2)), [w] "s"(w), [j] "s"(j), [lay] "s"(layer), [tag] "s"(tag),
            [shb] "s"(OFF_L0 + LO_H + c * 224 * 1024), [sk] "s"(w * 4096), [sxo] "s"(OFF_L0 + LO_X3 + ((2 * c + (w & 1)) * 32 + 4 * j + (w >> 1)) * 1024),
            [sso] "s"(OFF_L0 + LO_S3 + (c * 8 + j) * 2048 + w * 256), [sf] "s"(flag_off(SL_X3, c * 8 + j)), [mdu] "s"(US(lds0 + L_DUMMY))
          : "v0", "v1", "v2", "v3", "v4", "v5", "v6", "v7", "v8", "v9", "v10", "v11", "v12", "v13", "v14", "v15", "v16", "v17", "v18", "v19", "v20", "v21", "v22", "v23", "v24", "v25", "v26", "v27", "v28", "v29", "v30", "v31", "v32", "v33", "v34", "v35", "v36", "v37", "v38", "v39", "v40", "v41", "v42", "v43", "v44", "v45", "v46", "v47", "v48", "v49", "v50", "v51", "v52", "v53", "v54", "v55", "v56", "v57", "v58", "v59", "v60", "v61", "v62", "v63", "v64", "v65", "v66", "v67", "v68", "v69", "v70", "v71", "v72", "v73", "v74", "v75", "v76", "v77", "v78", "v79", "v80", "v81", "v82", "v83", "v112", "v113", "v114", "v115", "v116", "v117", "v118", "v119", "v120", "v121", "v122", "v123", "v124", "v125", "v126", "v127", "scc", "vcc", "memory");
    }
    if (layer == 0) {
      PHASE_VARS();
      const int mm = (w + j) & 7, tv = w < 4 ? 0 : (w >> 1) & 1;
      u32 sx = OFF_L0 + LO_X3 + 2 * c * 32768 + mm * 4096, sw = OFF_WQKV + qkv_tile(w, j) * 32768, t0;
      asm volatile(
          "s_nop 4\n\ts_waitcnt lgkmcnt(0)\n\ts_cmp_ge_u32 %[w], 4\n\ts_cbranch_scc1 Q0%=\n\t"
          "ds_read_b128 v[96:99], %[vx0] offset:0\n\tds_read_b128 v[100:103], %[vx0] offset:1024\n\tds_read_b128 v[104:107], %[vx0] offset:2048\n\tds_read_b128 v[108:111], %[vx0] offset:3072\n\t"
          "ds_read_b128 v[112:115], %[vx0] offset:4096\n\tds_read_b128 v[116:119], %[vx0] offset:5120\n\tds_read_b128 v[120:123], %[vx0] offset:6144\n\tds_read_b128 v[124:127], %[vx0] offset:7168\n\t"
          "s_waitcnt lgkmcnt(7)\n\tv_mfma_f32_32x32x16_bf16 v[0:15], v[128:131], v[96:99], 0\n\ts_waitcnt lgkmcnt(6)\n\tv_mfma_f32_32x32x16_bf16 v[16:31], v[128:131], v[100:103], 0\n\t"
          "s_waitcnt lgkmcnt(5)\n\tv_mfma_f32_32x32x16_bf16 v[0:15], v[132:135], v[104:107], v[0:15]\n\ts_waitcnt lgkmcnt(4)\n\tv_mfma_f32_32x32x16_bf16 v[16:31], v[132:135], v[108:111], v[16:31]\n\t"
          "s_waitcnt lgkmcnt(3)\n\tv_mfma_f32_32x32x16_bf16 v[0:15], v[136:139], v[112:115], v[0:15]\n\ts_waitcnt lgkmcnt(2)\n\tv_mfma_f32_32x32x16_bf16 v[16:31], v[136:139], v[116:119], v[16:31]\n\t"
          "s_waitcnt lgkmcnt(1)\n\tv_mfma_f32_32x32x16_bf16 v[0:15], v[140:143], v[120:123], v[0:15]\n\ts_waitcnt lgkmcnt(0)\n\tv_mfma_f32_32x32x16_bf16 v[16:31], v[140:143], v[124:127], v[16:31]\n\t"
          "s_branch Q1%=\n\tQ0%=:\n\tds_read_b128 v[96:99], %[vx0] offset:0\n\tds_read_b128 v[100:103], %[vx0] offset:2048\n\t"
          "ds_read_b128 v[104:107], %[vx0] offset:4096\n\tds_read_b128 v[108:111], %[vx0] offset:6144\n\ts_waitcnt lgkmcnt(3)\n\tv_mfma_f32_32x32x16_bf16 v[0:15], v[96:99], v[232:235], 0\n\t"
          "s_waitcnt lgkmcnt(2)\n\tv_mfma_f32_32x32x16_bf16 v[0:15], v[100:103], v[236:239], v[0:15]\n\ts_waitcnt lgkmcnt(1)\n\tv_mfma_f32_32x32x16_bf16 v[0:15], v[104:107], v[240:243], v[0:15]\n\t"
          "s_waitcnt lgkmcnt(0)\n\tv_mfma_f32_32x32x16_bf16 v[0:15], v[108:111], v[244:247], v[0:15]\n\tQ1%=:\n\ts_mov_b32 m0, %[ma]\n\t"
          "v_lshrrev_b32 v76, 1, %[l16]\n\ts_cmp_lt_u32 %[w], 4\n\ts_cbranch_scc1 QPO%=\n\tQPO%=:\n\t"
          "QPA%=:\n\tbuffer_load_dwordx2 v[68:69], %[vf], %[desc], 0 offen sc1\n\ts_waitcnt vmcnt(0)\n\tv_cmp_ne_u64 vcc, %[tag], v[68:69]\n\t"
          "s_cbranch_vccz QPB%=\n\ts_sub_u32 %[bud], %[bud], 1\n\ts_cbranch_scc0 QPA%=\n\ts_mov_b32 %[bud], 0\n\t"
          "QPB%=:\n\ts_cmp_eq_u32 %[w], 0\n\ts_cbranch_scc1 QPS%=\n\tbuffer_load_dwordx4 %[l16], %[desc], %[sx] offen offset:0 sc1 lds\n\t"
          "buffer_load_dwordx4 %[l16], %[desc], %[sx] offen offset:1024 sc1 lds\n\tbuffer_load_dwordx4 %[l16], %[desc], %[sx] offen offset:2048 sc1 lds\n\tbuffer_load_dwordx4 %[l16], %[desc], %[sx] offen offset:3072 sc1 lds\n\ts_add_u32 m0, m0, 32768\n\t"
          "s_add_u32 %[sx], %[sx], 32768\n\tbuffer_load_dwordx4 %[l16], %[desc], %[sx] offen offset:0 sc1 lds\n\tbuffer_load_dwordx4 %[l16], %[desc], %[sx] offen offset:1024 sc1 lds\n\tbuffer_load_dwordx4 %[l16], %[desc], %[sx] offen offset:2048 sc1 lds\n\t"
          "buffer_load_dwordx4 %[l16], %[desc], %[sx] offen offset:3072 sc1 lds\n\tQPS%=:\n\tbuffer_load_dwordx2 v[68:69], v76, %[desc], %[ss] offen offset:0 sc1\n\tbuffer_load_dwordx2 v[70:71], v76, %[desc], %[ss] offen offset:512 sc1\n\t"
          "buffer_load_dwordx2 v[72:73], v76, %[desc], %[ss] offen offset:1024 sc1\n\tbuffer_load_dwordx2 v[74:75], v76, %[desc], %[ss] offen offset:1536 sc1\n\ts_cmp_lt_u32 %[w], 4\n\ts_cbranch_scc1 QPR%=\n\t"
          "s_barrier\n\tQPR%=:\n\ts_waitcnt vmcnt(0)\n\tv_pk_add_f32 v[68:69], v[68:69], v[70:71]\n\t"
          "v_pk_add_f32 v[72:73], v[72:73], v[74:75]\n\tv_pk_add_f32 v[68:69], v[68:69], v[72:73]\n\tds_write_b64 %[vs], v[68:69]\n\ts_waitcnt lgkmcnt(0)\n\t"
          "s_barrier\n\ts_cmp_ge_u32 %[w], 4\n\ts_cbranch_scc1 QW%=\n\ts_setprio 2\n\t"
          "s_add_u32 %[sw], %[sw], 24576\n\tbuffer_load_dwordx4 v[224:227], %[l16], %[desc], %[sw] offen offset:0 sc1\n\tbuffer_load_dwordx4 v[228:231], %[l16], %[desc], %[sw] offen offset:1024 sc1\n\tbuffer_load_dwordx4 v[232:235], %[l16], %[desc], %[sw] offen offset:2048 sc1\n\t"
          "buffer_load_dwordx4 v[236:239], %[l16], %[desc], %[sw] offen offset:3072 sc1\n\ts_add_u32 %[sw], %[sw], 4096\n\tbuffer_load_dwordx4 v[240:243], %[l16], %[desc], %[sw] offen offset:0 sc1\n\tbuffer_load_dwordx4 v[244:247], %[l16], %[desc], %[sw] offen offset:1024 sc1\n\t"
          "buffer_load_dwordx4 v[248:251], %[l16], %[desc], %[sw] offen offset:2048 sc1\n\tbuffer_load_dwordx4 v[252:255], %[l16], %[desc], %[sw] offen offset:3072 sc1\n\tds_read_b128 v[96:99], %[vr] offset:4096\n\tds_read_b128 v[100:103], %[vr] offset:36864\n\t"
          "ds_read_b128 v[104:107], %[vr] offset:5120\n\tds_read_b128 v[108:111], %[vr] offset:37888\n\tds_read_b128 v[112:115], %[vr] offset:6144\n\tds_read_b128 v[116:119], %[vr] offset:38912\n\t"
          "ds_read_b128 v[120:123], %[vr] offset:7168\n\tds_read_b128 v[124:127], %[vr] offset:39936\n\ts_waitcnt lgkmcnt(7)\n\tv_mfma_f32_32x32x16_bf16 v[0:15], v[144:147], v[96:99], v[0:15]\n\t"
          "ds_read_b128 v[96:99], %[vr] offset:8192\n\ts_waitcnt lgkmcnt(7)\n\tv_mfma_f32_32x32x16_bf16 v[16:31], v[144:147], v[100:103], v[16:31]\n\tds_read_b128 v[100:103], %[vr] offset:40960\n\t"
          "s_waitcnt lgkmcnt(7)\n\tv_mfma_f32_32x32x16_bf16 v[0:15], v[148:151], v[104:107], v[0:15]\n\tds_read_b128 v[104:107], %[vr] offset:9216\n\ts_waitcnt lgkmcnt(7)\n\t"
          "v_mfma_f32_32x32x16_bf16 v[16:31], v[148:151], v[108:111], v[16:31]\n\tds_read_b128 v[108:111], %[vr] offset:41984\n\ts_waitcnt lgkmcnt(7)\n\tv_mfma_f32_32x32x16_bf16 v[0:15], v[152:155], v[112:115], v[0:15]\n\t"
          "ds_read_b128 v[112:115], %[vr] offset:10240\n\ts_waitcnt lgkmcnt(7)\n\tv_mfma_f32_32x32x16_bf16 v[16:31], v[152:155], v[116:119], v[16:31]\n\tds_read_b128 v[116:119], %[vr] offset:43008\n\t"
          "s_waitcnt lgkmcnt(7)\n\tv_mfma_f32_32x32x16_bf16 v[0:15], v[156:159], v[120:123], v[0:15]\n\tds_read_b128 v[120:123], %[vr] offset:11264\n\ts_waitcnt lgkmcnt(7)\n\t"
          "v_mfma_f32_32x32x16_bf16 v[16:31], v[156:159], v[124:127], v[16:31]\n\tds_read_b128 v[124:127], %[vr] offset:44032\n\ts_waitcnt lgkmcnt(7)\n\tv_mfma_f32_32x32x16_bf16 v[0:15], v[160:163], v[96:99], v[0:15]\n\t"
          "ds_read_b128 v[96:99], %[vr] offset:12288\n\ts_waitcnt lgkmcnt(7)\n\tv_mfma_f32_32x32x16_bf16 v[16:31], v[160:163], v[100:103], v[16:31]\n\tds_read_b128 v[100:103], %[vr] offset:45056\n\t"
          "s_waitcnt lgkmcnt(7)\n\tv_mfma_f32_32x32x16_bf16 v[0:15], v[164:167], v[104:107], v[0:15]\n\tds_read_b128 v[104:107], %[vr] offset:13312\n\ts_waitcnt lgkmcnt(7)\n\t"
          "v_mfma_f32_32x32x16_bf16 v[16:31], v[164:167], v[108:111], v[16:31]\n\tds_read_b128 v[108:111], %[vr] offset:46080\n\ts_waitcnt lgkmcnt(7)\n\tv_mfma_f32_32x32x16_bf16 v[0:15], v[168:171], v[112:115], v[0:15]\n\t"
          "ds_read_b128 v[112:115], %[vr] offset:14336\n\ts_waitcnt lgkmcnt(7)\n\tv_mfma_f32_32x32x16_bf16 v[16:31], v[168:171], v[116:119], v[16:31]\n\tds_read_b128 v[116:119], %[vr] offset:47104\n\t"
          "s_waitcnt lgkmcnt(7)\n\tv_mfma_f32_32x32x16_bf16 v[0:15], v[172:175], v[120:123], v[0:15]\n\tds_read_b128 v[120:123], %[vr] offset:15360\n\ts_waitcnt lgkmcnt(7)\n\t"
          "v_mfma_f32_32x32x16_bf16 v[16:31], v[172:175], v[124:127], v[16:31]\n\tds_read_b128 v[124:127], %[vr] offset:48128\n\ts_waitcnt lgkmcnt(7)\n\tv_mfma_f32_32x32x16_bf16 v[0:15], v[176:179], v[96:99], v[0:15]\n\t"
          "s_barrier\n\tds_read_b128 v[96:99], %[vr] offset:16384\n\ts_waitcnt lgkmcnt(7)\n\tv_mfma_f32_32x32x16_bf16 v[16:31], v[176:179], v[100:103], v[16:31]\n\t"
          "ds_read_b128 v[100:103], %[vr] offset:49152\n\ts_waitcnt lgkmcnt(7)\n\tv_mfma_f32_32x32x16_bf16 v[0:15], v[180:183], v[104:107], v[0:15]\n\tds_read_b128 v[104:107], %[vr] offset:17408\n\t"
          "ds_read_b128 v[32:35], %[vst] offset:0\n\ts_waitcnt lgkmcnt(8)\n\tv_mfma_f32_32x32x16_bf16 v[16:31], v[180:183], v[108:111], v[16:31]\n\tds_read_b128 v[108:111], %[vr] offset:50176\n\t"
          "ds_read_b128 v[36:39], %[vst] offset:512\n\ts_waitcnt lgkmcnt(9)\n\tv_mfma_f32_32x32x16_bf16 v[0:15], v[184:187], v[112:115], v[0:15]\n\tds_read_b128 v[112:115], %[vr] offset:18432\n\t"
          "ds_read_b128 v[40:43], %[vst] offset:1024\n\ts_waitcnt lgkmcnt(10)\n\tv_mfma_f32_32x32x16_bf16 v[16:31], v[184:187], v[116:119], v[16:31]\n\tds_read_b128 v[116:119], %[vr] offset:51200\n\t"
          "ds_read_b128 v[44:47], %[vst] offset:1536\n\ts_waitcnt lgkmcnt(11)\n\tv_mfma_f32_32x32x16_bf16 v[0:15], v[188:191], v[120:123], v[0:15]\n\tds_read_b128 v[120:123], %[vr] offset:19456\n\t"
          "ds_read_b128 v[48:51], %[vst] offset:2048\n\ts_waitcnt lgkmcnt(12)\n\tv_mfma_f32_32x32x16_bf16 v[16:31], v[188:191], v[124:127], v[16:31]\n\tds_read_b128 v[124:127], %[vr] offset:52224\n\t"
          "ds_read_b128 v[52:55], %[vst] offset:2560\n\ts_waitcnt lgkmcnt(13)\n\tv_mfma_f32_32x32x16_bf16 v[0:15], v[192:195], v[96:99], v[0:15]\n\tds_read_b128 v[96:99], %[vr] offset:20480\n\t"
          "s_waitcnt lgkmcnt(13)\n\tds_read_b128 v[56:59], %[vst] offset:3072\n\tv_mfma_f32_32x32x16_bf16 v[16:31], v[192:195], v[100:103], v[16:31]\n\tds_read_b128 v[100:103], %[vr] offset:53248\n\t"
          "s_waitcnt lgkmcnt(13)\n\tds_read_b128 v[60:63], %[vst] offset:3584\n\ts_waitcnt lgkmcnt(12)\n\tv_add_f32 v32, v32, v36\n\t"
          "v_mfma_f32_32x32x16_bf16 v[0:15], v[196:199], v[104:107], v[0:15]\n\tds_read_b128 v[104:107], %[vr] offset:21504\n\tv_add_f32 v33, v33, v37\n\tv_add_f32 v34, v34, v38\n\t"
          "v_mfma_f32_32x32x16_bf16 v[16:31], v[196:199], v[108:111], v[16:31]\n\tds_read_b128 v[108:111], %[vr] offset:54272\n\tv_add_f32 v35, v35, v39\n\ts_waitcnt lgkmcnt(10)\n\t"
          "v_add_f32 v40, v40, v44\n\tv_mfma_f32_32x32x16_bf16 v[0:15], v[200:203], v[112:115], v[0:15]\n\tds_read_b128 v[112:115], %[vr] offset:22528\n\tv_add_f32 v41, v41, v45\n\t"
          "v_add_f32 v42, v42, v46\n\tv_mfma_f32_32x32x16_bf16 v[16:31], v[200:203], v[116:119], v[16:31]\n\tds_read_b128 v[116:119], %[vr] offset:55296\n\tv_add_f32 v43, v43, v47\n\t"
          "s_waitcnt lgkmcnt(8)\n\tv_add_f32 v48, v48, v52\n\tv_mfma_f32_32x32x16_bf16 v[0:15], v[204:207], v[120:123], v[0:15]\n\tds_read_b128 v[120:123], %[vr] offset:23552\n\t"
          "v_add_f32 v49, v49, v53\n\tv_add_f32 v50, v50, v54\n\tv_mfma_f32_32x32x16_bf16 v[16:31], v[204:207], v[124:127], v[16:31]\n\tds_read_b128 v[124:127], %[vr] offset:56320\n\t"
          "v_add_f32 v51, v51, v55\n\ts_waitcnt lgkmcnt(6)\n\tv_add_f32 v56, v56, v60\n\tv_mfma_f32_32x32x16_bf16 v[0:15], v[208:211], v[96:99], v[0:15]\n\t"
          "ds_read_b128 v[96:99], %[vr] offset:24576\n\tv_add_f32 v57, v57, v61\n\tv_add_f32 v58, v58, v62\n\tv_mfma_f32_32x32x16_bf16 v[16:31], v[208:211], v[100:103], v[16:31]\n\t"
          "ds_read_b128 v[100:103], %[vr] offset:57344\n\tv_add_f32 v59, v59, v63\n\tv_add_f32 v32, v32, v40\n\ts_waitcnt lgkmcnt(7)\n\t"
          "v_mfma_f32_32x32x16_bf16 v[0:15], v[212:215], v[104:107], v[0:15]\n\tds_read_b128 v[104:107], %[vr] offset:25600\n\tv_add_f32 v33, v33, v41\n\tv_add_f32 v34, v34, v42\n\t"
          "s_waitcnt lgkmcnt(7)\n\tv_mfma_f32_32x32x16_bf16 v[16:31], v[212:215], v[108:111], v[16:31]\n\tds_read_b128 v[108:111], %[vr] offset:58368\n\tv_add_f32 v35, v35, v43\n\t"
          "v_add_f32 v48, v48, v56\n\ts_waitcnt lgkmcnt(7)\n\tv_mfma_f32_32x32x16_bf16 v[0:15], v[216:219], v[112:115], v[0:15]\n\tds_read_b128 v[112:115], %[vr] offset:26624\n\t"
          "v_add_f32 v49, v49, v57\n\tv_add_f32 v50, v50, v58\n\ts_waitcnt lgkmcnt(7)\n\tv_mfma_f32_32x32x16_bf16 v[16:31], v[216:219], v[116:119], v[16:31]\n\t"
          "ds_read_b128 v[116:119], %[vr] offset:59392\n\tv_add_f32 v51, v51, v59\n\tv_add_f32 v32, v32, v48\n\ts_waitcnt lgkmcnt(7)\n\t"
          "v_mfma_f32_32x32x16_bf16 v[0:15], v[220:223], v[120:123], v[0:15]\n\tds_read_b128 v[120:123], %[vr] offset:27648\n\tv_add_f32 v33, v33, v49\n\tv_add_f32 v34, v34, v50\n\t"
          "s_waitcnt lgkmcnt(7)\n\tv_mfma_f32_32x32x16_bf16 v[16:31], v[220:223], v[124:127], v[16:31]\n\tds_read_b128 v[124:127], %[vr] offset:60416\n\tv_add_f32 v35, v35, v51\n\t"
          "v_mul_f32 v64, 0xbb000000, v32\n\ts_waitcnt vmcnt(7)\n\ts_waitcnt lgkmcnt(7)\n\tv_mfma_f32_32x32x16_bf16 v[0:15], v[224:227], v[96:99], v[0:15]\n\t"
          "ds_read_b128 v[96:99], %[vr] offset:28672\n\tv_mul_f32 v66, 0xbb000000, v34\n\tv_mul_f32 v68, 0x3b000000, v33\n\ts_waitcnt lgkmcnt(7)\n\t"
          "v_mfma_f32_32x32x16_bf16 v[16:31], v[224:227], v[100:103], v[16:31]\n\tds_read_b128 v[100:103], %[vr] offset:61440\n\tv_mul_f32 v69, 0x3b000000, v35\n\tv_fma_f32 v68, -v64, v64, v68\n\t"
          "s_waitcnt vmcnt(6)\n\ts_waitcnt lgkmcnt(7)\n\tv_mfma_f32_32x32x16_bf16 v[0:15], v[228:231], v[104:107], v[0:15]\n\tds_read_b128 v[104:107], %[vr] offset:29696\n\t"
          "v_fma_f32 v69, -v66, v66, v69\n\tv_max_f32 v68, 0, v68\n\ts_waitcnt lgkmcnt(7)\n\tv_mfma_f32_32x32x16_bf16 v[16:31], v[228:231], v[108:111], v[16:31]\n\t"
          "ds_read_b128 v[108:111], %[vr] offset:62464\n\tv_max_f32 v69, 0, v69\n\tv_add_f32 v70, 0x3727c5ac, v68\n\ts_waitcnt vmcnt(5)\n\t"
          "s_waitcnt lgkmcnt(7)\n\tv_mfma_f32_32x32x16_bf16 v[0:15], v[232:235], v[112:115], v[0:15]\n\tds_read_b128 v[112:115], %[vr] offset:30720\n\tv_add_f32 v71, 0x3727c5ac, v69\n\t"
          "s_waitcnt lgkmcnt(7)\n\tv_mfma_f32_32x32x16_bf16 v[16:31], v[232:235], v[116:119], v[16:31]\n\tds_read_b128 v[116:119], %[vr] offset:63488\n\tv_rsq_f32 v65, v70\n\t"
          "s_waitcnt vmcnt(4)\n\ts_waitcnt lgkmcnt(7)\n\tv_mfma_f32_32x32x16_bf16 v[0:15], v[236:239], v[120:123], v[0:15]\n\tds_read_b128 v[120:123], %[vr] offset:31744\n\t"
          "v_rsq_f32 v67, v71\n\ts_waitcnt lgkmcnt(7)\n\tv_mfma_f32_32x32x16_bf16 v[16:31], v[236:239], v[124:127], v[16:31]\n\tds_read_b128 v[124:127], %[vr] offset:64512\n\t"
          "s_nop 0\n\tds_read_b128 v[32:35], %[vtab] offset:0\n\ts_waitcnt vmcnt(3)\n\ts_waitcnt lgkmcnt(8)\n\t"
          "v_mfma_f32_32x32x16_bf16 v[0:15], v[240:243], v[96:99], v[0:15]\n\tds_read_b128 v[36:39], %[vtab] offset:32\n\ts_waitcnt lgkmcnt(8)\n\tv_mfma_f32_32x32x16_bf16 v[16:31], v[240:243], v[100:103], v[16:31]\n\t"
          "ds_read_b128 v[40:43], %[vtab] offset:64\n\ts_waitcnt vmcnt(2)\n\ts_waitcnt lgkmcnt(8)\n\tv_mfma_f32_32x32x16_bf16 v[0:15], v[244:247], v[104:107], v[0:15]\n\t"
          "ds_read_b128 v[44:47], %[vtab] offset:96\n\tv_mul_f32 v65, %[sc], v65\n\ts_waitcnt lgkmcnt(8)\n\tv_mfma_f32_32x32x16_bf16 v[16:31], v[244:247], v[108:111], v[16:31]\n\t"
          "v_mul_f32 v67, %[sc], v67\n\tv_add_u32 v84, %[unit], %[vo]\n\ts_waitcnt vmcnt(1)\n\ts_waitcnt lgkmcnt(7)\n\t"
          "v_mfma_f32_32x32x16_bf16 v[0:15], v[248:251], v[112:115], v[0:15]\n\ts_lshl_b32 %[t0], %[unit], 6\n\ts_waitcnt lgkmcnt(6)\n\tv_mfma_f32_32x32x16_bf16 v[16:31], v[248:251], v[116:119], v[16:31]\n\t"
          "s_waitcnt vmcnt(0)\n\ts_waitcnt lgkmcnt(5)\n\tv_mfma_f32_32x32x16_bf16 v[0:15], v[252:255], v[120:123], v[0:15]\n\ts_waitcnt lgkmcnt(0)\n\t"
          "v_mfma_f32_32x32x16_bf16 v[16:31], v[252:255], v[124:127], v[16:31]\n\ts_nop 10\n\tv_fmac_f32 v0, v64, v32\n\tv_fmac_f32 v1, v64, v33\n\t"
          "v_fmac_f32 v2, v64, v34\n\tv_fmac_f32 v3, v64, v35\n\tv_fmac_f32 v4, v64, v36\n\tv_fmac_f32 v5, v64, v37\n\t"
          "v_fmac_f32 v6, v64, v38\n\tv_fmac_f32 v7, v64, v39\n\tv_fmac_f32 v8, v64, v40\n\tv_fmac_f32 v9, v64, v41\n\t"
          "v_fmac_f32 v10, v64, v42\n\tv_fmac_f32 v11, v64, v43\n\tv_fmac_f32 v12, v64, v44\n\tv_fmac_f32 v13, v64, v45\n\t"
          "v_fmac_f32 v14, v64, v46\n\tv_fmac_f32 v15, v64, v47\n\tv_mul_f32 v0, v65, v0\n\tv_mul_f32 v1, v65, v1\n\t"
          "v_mul_f32 v2, v65, v2\n\tv_mul_f32 v3, v65, v3\n\tv_mul_f32 v4, v65, v4\n\tv_mul_f32 v5, v65, v5\n\t"
          "v_mul_f32 v6, v65, v6\n\tv_mul_f32 v7, v65, v7\n\tv_mul_f32 v8, v65, v8\n\tv_mul_f32 v9, v65, v9\n\t"
          "v_mul_f32 v10, v65, v10\n\tv_mul_f32 v11, v65, v11\n\tv_mul_f32 v12, v65, v12\n\tv_mul_f32 v13, v65, v13\n\t"
          "v_mul_f32 v14, v65, v14\n\tv_mul_f32 v15, v65, v15\n\tv_cvt_pk_bf16_f32 v68, v0, v1\n\tv_cvt_pk_bf16_f32 v69, v2, v3\n\t"
          "v_cvt_pk_bf16_f32 v70, v4, v5\n\tv_cvt_pk_bf16_f32 v71, v6, v7\n\tv_cvt_pk_bf16_f32 v72, v8, v9\n\tv_cvt_pk_bf16_f32 v73, v10, v11\n\t"
          "v_cvt_pk_bf16_f32 v74, v12, v13\n\tv_cvt_pk_bf16_f32 v75, v14, v15\n\tbuffer_store_dwordx4 v[68:71], %[vo], %[desc], 0 offen sc1\n\tbuffer_store_dwordx4 v[72:75], %[vo], %[desc], %[t0] offen sc1\n\t"
          "v_fmac_f32 v16, v66, v32\n\tv_fmac_f32 v17, v66, v33\n\tv_fmac_f32 v18, v66, v34\n\tv_fmac_f32 v19, v66, v35\n\t"
          "v_fmac_f32 v20, v66, v36\n\tv_fmac_f32 v21, v66, v37\n\tv_fmac_f32 v22, v66, v38\n\tv_fmac_f32 v23, v66, v39\n\t"
          "v_fmac_f32 v24, v66, v40\n\tv_fmac_f32 v25, v66, v41\n\tv_fmac_f32 v26, v66, v42\n\tv_fmac_f32 v27, v66, v43\n\t"
          "v_fmac_f32 v28, v66, v44\n\tv_fmac_f32 v29, v66, v45\n\tv_fmac_f32 v30, v66, v46\n\tv_fmac_f32 v31, v66, v47\n\t"
          "v_mul_f32 v16, v67, v16\n\tv_mul_f32 v17, v67, v17\n\tv_mul_f32 v18, v67, v18\n\tv_mul_f32 v19, v67, v19\n\t"
          "v_mul_f32 v20, v67, v20\n\tv_mul_f32 v21, v67, v21\n\tv_mul_f32 v22, v67, v22\n\tv_mul_f32 v23, v67, v23\n\t"
          "v_mul_f32 v24, v67, v24\n\tv_mul_f32 v25, v67, v25\n\tv_mul_f32 v26, v67, v26\n\tv_mul_f32 v27, v67, v27\n\t"
          "v_mul_f32 v28, v67, v28\n\tv_mul_f32 v29, v67, v29\n\tv_mul_f32 v30, v67, v30\n\tv_mul_f32 v31, v67, v31\n\t"
          "v_cvt_pk_bf16_f32 v76, v16, v17\n\tv_cvt_pk_bf16_f32 v77, v18, v19\n\tv_cvt_pk_bf16_f32 v78, v20, v21\n\tv_cvt_pk_bf16_f32 v79, v22, v23\n\t"
          "v_cvt_pk_bf16_f32 v80, v24, v25\n\tv_cvt_pk_bf16_f32 v81, v26, v27\n\tv_cvt_pk_bf16_f32 v82, v28, v29\n\tv_cvt_pk_bf16_f32 v83, v30, v31\n\t"
          "buffer_store_dwordx4 v[76:79], v84, %[desc], 0 offen sc1\n\tbuffer_store_dwordx4 v[80:83], v84, %[desc], %[t0] offen sc1\n\ts_branch QX%=\n\tQW%=:\n\t"
          "s_add_u32 %[sw], %[sw], 14336\n\tbuffer_load_dwordx4 v[160:163], %[l16], %[desc], %[sw] offen offset:0 sc1\n\tbuffer_load_dwordx4 v[164:167], %[l16], %[desc], %[sw] offen offset:1024 sc1\n\tbuffer_load_dwordx4 v[168:171], %[l16], %[desc], %[sw] offen offset:2048 sc1\n\t"
          "buffer_load_dwordx4 v[172:175], %[l16], %[desc], %[sw] offen offset:3072 sc1\n\ts_add_u32 %[sw], %[sw], 4096\n\tbuffer_load_dwordx4 v[176:179], %[l16], %[desc], %[sw] offen offset:0 sc1\n\tbuffer_load_dwordx4 v[180:183], %[l16], %[desc], %[sw] offen offset:1024 sc1\n\t"
          "buffer_load_dwordx4 v[184:187], %[l16], %[desc], %[sw] offen offset:2048 sc1\n\tbuffer_load_dwordx4 v[188:191], %[l16], %[desc], %[sw] offen offset:3072 sc1\n\ts_add_u32 %[sw], %[sw], 4096\n\tbuffer_load_dwordx4 v[192:195], %[l16], %[desc], %[sw] offen offset:0 sc1\n\t"
          "buffer_load_dwordx4 v[196:199], %[l16], %[desc], %[sw] offen offset:1024 sc1\n\tbuffer_load_dwordx4 v[200:203], %[l16], %[desc], %[sw] offen offset:2048 sc1\n\tbuffer_load_dwordx4 v[204:207], %[l16], %[desc], %[sw] offen offset:3072 sc1\n\ts_add_u32 %[sw], %[sw], 4096\n\t"
          "buffer_load_dwordx4 v[208:211], %[l16], %[desc], %[sw] offen offset:0 sc1\n\tbuffer_load_dwordx4 v[212:215], %[l16], %[desc], %[sw] offen offset:1024 sc1\n\tbuffer_load_dwordx4 v[216:219], %[l16], %[desc], %[sw] offen offset:2048 sc1\n\tbuffer_load_dwordx4 v[220:223], %[l16], %[desc], %[sw] offen offset:3072 sc1\n\t"
          "s_add_u32 %[sw], %[sw], 4096\n\tbuffer_load_dwordx4 v[224:227], %[l16], %[desc], %[sw] offen offset:0 sc1\n\tbuffer_load_dwordx4 v[228:231], %[l16], %[desc], %[sw] offen offset:1024 sc1\n\ts_setprio 3\n\t"
          "ds_read_b128 v[32:35], %[vst] offset:0\n\tds_read_b128 v[36:39], %[vst] offset:512\n\tds_read_b128 v[40:43], %[vst] offset:1024\n\tds_read_b128 v[44:47], %[vst] offset:1536\n\t"
          "ds_read_b128 v[48:51], %[vst] offset:2048\n\tds_read_b128 v[52:55], %[vst] offset:2560\n\tds_read_b128 v[56:59], %[vst] offset:3072\n\tds_read_b128 v[60:63], %[vst] offset:3584\n\t"
          "s_waitcnt lgkmcnt(6)\n\tv_add_f32 v32, v32, v36\n\tv_add_f32 v33, v33, v37\n\tv_add_f32 v34, v34, v38\n\t"
          "v_add_f32 v35, v35, v39\n\ts_waitcnt lgkmcnt(4)\n\tv_add_f32 v40, v40, v44\n\tv_add_f32 v41, v41, v45\n\t"
          "v_add_f32 v42, v42, v46\n\tv_add_f32 v43, v43, v47\n\ts_waitcnt lgkmcnt(2)\n\tv_add_f32 v48, v48, v52\n\t"
          "v_add_f32 v49, v49, v53\n\tv_add_f32 v50, v50, v54\n\tv_add_f32 v51, v51, v55\n\ts_waitcnt lgkmcnt(0)\n\t"
          "v_add_f32 v56, v56, v60\n\tv_add_f32 v57, v57, v61\n\tv_add_f32 v58, v58, v62\n\tv_add_f32 v59, v59, v63\n\t"
          "v_add_f32 v32, v32, v40\n\tv_add_f32 v33, v33, v41\n\tv_add_f32 v34, v34, v42\n\tv_add_f32 v35, v35, v43\n\t"
          "v_add_f32 v48, v48, v56\n\tv_add_f32 v49, v49, v57\n\tv_add_f32 v50, v50, v58\n\tv_add_f32 v51, v51, v59\n\t"
          "v_add_f32 v32, v32, v48\n\tv_add_f32 v33, v33, v49\n\tv_add_f32 v34, v34, v50\n\tv_add_f32 v35, v35, v51\n\t"
          "v_mul_f32 v64, 0xbb000000, v32\n\tv_mul_f32 v66, 0xbb000000, v34\n\tv_mul_f32 v68, 0x3b000000, v33\n\tv_mul_f32 v69, 0x3b000000, v35\n\t"
          "v_fma_f32 v68, -v64, v64, v68\n\tv_fma_f32 v69, -v66, v66, v69\n\tv_max_f32 v68, 0, v68\n\tv_max_f32 v69, 0, v69\n\t"
          "v_add_f32 v70, 0x3727c5ac, v68\n\tv_add_f32 v71, 0x3727c5ac, v69\n\tv_rsq_f32 v65, v70\n\tv_rsq_f32 v67, v71\n\t"
          "s_nop 0\n\ts_bitcmp0_b32 %[w], 1\n\ts_cbranch_scc1 QS%=\n\tv_mov_b32 v64, v66\n\t"
          "v_mov_b32 v65, v67\n\tQS%=:\n\tds_write_b32 %[vx], v64 offset:384\n\tds_write_b32 %[vx], v65 offset:512\n\t"
          "ds_read_b128 v[32:35], %[vtab] offset:384\n\tds_read_b128 v[36:39], %[vtab] offset:416\n\tds_read_b128 v[40:43], %[vtab] offset:448\n\tds_read_b128 v[44:47], %[vtab] offset:480\n\t"
          "ds_read_b128 v[48:51], %[vtab] offset:512\n\tds_read_b128 v[52:55], %[vtab] offset:544\n\tds_read_b128 v[56:59], %[vtab] offset:576\n\tds_read_b128 v[60:63], %[vtab] offset:608\n\t"
          "ds_read_b32 v64, %[vx]\n\ts_waitcnt lgkmcnt(0)\n\ts_setprio 1\n\tds_read_b128 v[96:99], %[vr] offset:4096\n\t"
          "ds_read_b128 v[100:103], %[vr] offset:5120\n\tds_read_b128 v[104:107], %[vr] offset:6144\n\tds_read_b128 v[108:111], %[vr] offset:7168\n\tds_read_b128 v[112:115], %[vr] offset:8192\n\t"
          "ds_read_b128 v[116:119], %[vr] offset:9216\n\tds_read_b128 v[120:123], %[vr] offset:10240\n\tds_read_b128 v[124:127], %[vr] offset:11264\n\ts_waitcnt lgkmcnt(7)\n\t"
          "v_mfma_f32_32x32x16_bf16 v[0:15], v[96:99], v[248:251], v[0:15]\n\tds_read_b128 v[96:99], %[vr] offset:12288\n\ts_waitcnt lgkmcnt(7)\n\tv_mfma_f32_32x32x16_bf16 v[0:15], v[100:103], v[252:255], v[0:15]\n\t"
          "ds_read_b128 v[100:103], %[vr] offset:13312\n\ts_waitcnt lgkmcnt(7)\n\tv_mfma_f32_32x32x16_bf16 v[0:15], v[104:107], v[128:131], v[0:15]\n\tds_read_b128 v[104:107], %[vr] offset:14336\n\t"
          "s_waitcnt lgkmcnt(7)\n\tv_mfma_f32_32x32x16_bf16 v[0:15], v[108:111], v[132:135], v[0:15]\n\tds_read_b128 v[108:111], %[vr] offset:15360\n\ts_waitcnt lgkmcnt(7)\n\t"
          "v_mfma_f32_32x32x16_bf16 v[0:15], v[112:115], v[136:139], v[0:15]\n\tds_read_b128 v[112:115], %[vr] offset:16384\n\ts_waitcnt lgkmcnt(7)\n\tv_mfma_f32_32x32x16_bf16 v[0:15], v[116:119], v[140:143], v[0:15]\n\t"
          "ds_read_b128 v[116:119], %[vr] offset:17408\n\ts_waitcnt lgkmcnt(7)\n\tv_mfma_f32_32x32x16_bf16 v[0:15], v[120:123], v[144:147], v[0:15]\n\tds_read_b128 v[120:123], %[vr] offset:18432\n\t"
          "s_waitcnt lgkmcnt(7)\n\tv_mfma_f32_32x32x16_bf16 v[0:15], v[124:127], v[148:151], v[0:15]\n\tds_read_b128 v[124:127], %[vr] offset:19456\n\ts_waitcnt lgkmcnt(7)\n\t"
          "v_mfma_f32_32x32x16_bf16 v[0:15], v[96:99], v[152:155], v[0:15]\n\tds_read_b128 v[96:99], %[vr] offset:20480\n\ts_waitcnt lgkmcnt(7)\n\tv_mfma_f32_32x32x16_bf16 v[0:15], v[100:103], v[156:159], v[0:15]\n\t"
          "ds_read_b128 v[100:103], %[vr] offset:21504\n\ts_waitcnt vmcnt(17)\n\ts_waitcnt lgkmcnt(7)\n\tv_mfma_f32_32x32x16_bf16 v[0:15], v[104:107], v[160:163], v[0:15]\n\t"
          "ds_read_b128 v[104:107], %[vr] offset:22528\n\ts_waitcnt vmcnt(16)\n\ts_waitcnt lgkmcnt(7)\n\tv_mfma_f32_32x32x16_bf16 v[0:15], v[108:111], v[164:167], v[0:15]\n\t"
          "ds_read_b128 v[108:111], %[vr] offset:23552\n\ts_waitcnt vmcnt(15)\n\ts_waitcnt lgkmcnt(7)\n\tv_mfma_f32_32x32x16_bf16 v[0:15], v[112:115], v[168:171], v[0:15]\n\t"
          "ds_read_b128 v[112:115], %[vr] offset:24576\n\ts_waitcnt vmcnt(14)\n\ts_waitcnt lgkmcnt(7)\n\tv_mfma_f32_32x32x16_bf16 v[0:15], v[116:119], v[172:175], v[0:15]\n\t"
          "ds_read_b128 v[116:119], %[vr] offset:25600\n\ts_waitcnt vmcnt(13)\n\ts_waitcnt lgkmcnt(7)\n\tv_mfma_f32_32x32x16_bf16 v[0:15], v[120:123], v[176:179], v[0:15]\n\t"
          "ds_read_b128 v[120:123], %[vr] offset:26624\n\ts_waitcnt vmcnt(12)\n\ts_waitcnt lgkmcnt(7)\n\tv_mfma_f32_32x32x16_bf16 v[0:15], v[124:127], v[180:183], v[0:15]\n\t"
          "ds_read_b128 v[124:127], %[vr] offset:27648\n\ts_waitcnt vmcnt(11)\n\ts_waitcnt lgkmcnt(7)\n\tv_mfma_f32_32x32x16_bf16 v[0:15], v[96:99], v[184:187], v[0:15]\n\t"
          "ds_read_b128 v[96:99], %[vr] offset:28672\n\ts_waitcnt vmcnt(10)\n\ts_waitcnt lgkmcnt(7)\n\tv_mfma_f32_32x32x16_bf16 v[0:15], v[100:103], v[188:191], v[0:15]\n\t"
          "ds_read_b128 v[100:103], %[vr] offset:29696\n\ts_waitcnt vmcnt(9)\n\ts_waitcnt lgkmcnt(7)\n\tv_mfma_f32_32x32x16_bf16 v[0:15], v[104:107], v[192:195], v[0:15]\n\t"
          "ds_read_b128 v[104:107], %[vr] offset:30720\n\ts_waitcnt vmcnt(8)\n\ts_waitcnt lgkmcnt(7)\n\tv_mfma_f32_32x32x16_bf16 v[0:15], v[108:111], v[196:199], v[0:15]\n\t"
          "ds_read_b128 v[108:111], %[vr] offset:31744\n\ts_waitcnt vmcnt(7)\n\ts_waitcnt lgkmcnt(7)\n\tv_mfma_f32_32x32x16_bf16 v[0:15], v[112:115], v[200:203], v[0:15]\n\t"
          "s_waitcnt vmcnt(6)\n\ts_waitcnt lgkmcnt(6)\n\tv_mfma_f32_32x32x16_bf16 v[0:15], v[116:119], v[204:207], v[0:15]\n\ts_waitcnt vmcnt(5)\n\t"
          "s_waitcnt lgkmcnt(5)\n\tv_mfma_f32_32x32x16_bf16 v[0:15], v[120:123], v[208:211], v[0:15]\n\ts_waitcnt vmcnt(4)\n\ts_waitcnt lgkmcnt(4)\n\t"
          "v_mfma_f32_32x32x16_bf16 v[0:15], v[124:127], v[212:215], v[0:15]\n\ts_waitcnt vmcnt(3)\n\ts_waitcnt lgkmcnt(3)\n\tv_mfma_f32_32x32x16_bf16 v[0:15], v[96:99], v[216:219], v[0:15]\n\t"
          "s_waitcnt vmcnt(2)\n\ts_waitcnt lgkmcnt(2)\n\tv_mfma_f32_32x32x16_bf16 v[0:15], v[100:103], v[220:223], v[0:15]\n\ts_waitcnt vmcnt(1)\n\t"
          "s_waitcnt lgkmcnt(1)\n\tv_mfma_f32_32x32x16_bf16 v[0:15], v[104:107], v[224:227], v[0:15]\n\ts_waitcnt vmcnt(0)\n\ts_waitcnt lgkmcnt(0)\n\t"
          "v_mfma_f32_32x32x16_bf16 v[0:15], v[108:111], v[228:231], v[0:15]\n\ts_nop 15\n\tv_pk_fma_f32 v[0:1], v[64:65], v[32:33], v[0:1] op_sel_hi:[0,1,1]\n\tv_pk_fma_f32 v[2:3], v[64:65], v[34:35], v[2:3] op_sel_hi:[0,1,1]\n\t"
          "v_pk_fma_f32 v[4:5], v[64:65], v[36:37], v[4:5] op_sel_hi:[0,1,1]\n\tv_pk_fma_f32 v[6:7], v[64:65], v[38:39], v[6:7] op_sel_hi:[0,1,1]\n\tv_pk_fma_f32 v[8:9], v[64:65], v[40:41], v[8:9] op_sel_hi:[0,1,1]\n\tv_pk_fma_f32 v[10:11], v[64:65], v[42:43], v[10:11] op_sel_hi:[0,1,1]\n\t"
          "v_pk_fma_f32 v[12:13], v[64:65], v[44:45], v[12:13] op_sel_hi:[0,1,1]\n\tv_pk_fma_f32 v[14:15], v[64:65], v[46:47], v[14:15] op_sel_hi:[0,1,1]\n\tv_pk_mul_f32 v[0:1], v[48:49], v[0:1]\n\tv_pk_mul_f32 v[2:3], v[50:51], v[2:3]\n\t"
          "v_pk_mul_f32 v[4:5], v[52:53], v[4:5]\n\tv_pk_mul_f32 v[6:7], v[54:55], v[6:7]\n\tv_pk_mul_f32 v[8:9], v[56:57], v[8:9]\n\tv_pk_mul_f32 v[10:11], v[58:59], v[10:11]\n\t"
          "v_pk_mul_f32 v[12:13], v[60:61], v[12:13]\n\tv_pk_mul_f32 v[14:15], v[62:63], v[14:15]\n\tv_cvt_pk_bf16_f32 v68, v0, v1\n\tv_cvt_pk_bf16_f32 v69, v2, v3\n\t"
          "v_cvt_pk_bf16_f32 v70, v4, v5\n\tv_cvt_pk_bf16_f32 v71, v6, v7\n\tv_cvt_pk_bf16_f32 v72, v8, v9\n\tv_cvt_pk_bf16_f32 v73, v10, v11\n\t"
          "v_cvt_pk_bf16_f32 v74, v12, v13\n\tv_cvt_pk_bf16_f32 v75, v14, v15\n\tbuffer_store_dwordx4 v[68:71], %[vo], %[desc], 0 offen sc1\n\tbuffer_store_dwordx4 v[72:75], %[vo], %[desc], 0 offen offset:512 sc1\n\t"
          "QX%=:\n\ts_setprio 0\n\tv_mov_b32 v32, %[mfl]\n\tv_mov_b32 v33, 0\n\t"
          "ds_write_b32 v32, v33\n\tv_mov_b64 v[36:37], %[tag]\n\tv_mov_b64 v[38:39], %[tag]\n\ts_waitcnt vmcnt(0) lgkmcnt(0)\n\t"
          "s_barrier\n\ts_cmp_lg_u32 %[w], 0\n\ts_cbranch_scc1 QQ%=\n\ts_mov_b64 exec, 0xff\n\t"
          "buffer_store_dwordx4 v[36:39], %[l16], %[desc], %[sf] offen sc1\n\ts_mov_b64 exec, -1\n\ts_nop 1\n\tQQ%=:\n\t"
          "s_nop 7\n\t"
          : [sx] "+s"(sx), [sw] "+s"(sw), [bud] "+s"(bud), [t0] "=&s"(t0)
          : [l16] "v"(l16), [vx0] "v"(lds0 + L_XRES + tv * 1024 + l16), [vr] "v"(lds0 + L_ACT + tv * 32768 + l16), [vs] "v"(lds0 + L_STAT + w * 512 + tok * 16 + h * 8),
            [vst] "v"(lds0 + L_STAT + tok * 16), [vtab] "v"(ltab + h * 16), [vx] "v"(ltab + tok * 4), [vo] "v"(qkv_out(OFF_L0 + L_STRIDE, w, c, j, lane)),
            [vf] "v"(flag_off(SL_X3, c * 8 + mm) + (lane >> 6)),
            [desc] "s"(desc), [ss] "s"(OFF_L0 + LO_S3 + (c * 8 + mm) * 2048), [w] "s"(w), [tag] "s"(tag), [ma] "s"(US(lds0 + L_ACT + w * 4096)),
            [unit] "s"(w < 2 ? 1024 : 2048), [sc] "s"(w < 2 ? QSCALE : 1.f), [mfl] "s"(US(lds0 + L_FLAG)), [sf] "s"(flag_off(SL_QKV + 5, j * 32 + c))
          : "v0", "v1", "v2", "v3", "v4", "v5", "v6", "v7", "v8", "v9", "v10", "v11", "v12", "v13", "v14", "v15", "v16", "v17", "v18", "v19", "v20", "v21", "v22", "v23", "v24", "v25", "v26", "v27", "v28", "v29", "v30", "v31", "v32", "v33", "v34", "v35", "v36", "v37", "v38", "v39", "v40", "v41", "v42", "v43", "v44", "v45", "v46", "v47", "v48", "v49", "v50", "v51", "v52", "v53", "v54", "v55", "v56", "v57", "v58", "v59", "v60", "v61", "v62", "v63", "v64", "v65", "v66", "v67", "v68", "v69", "v70", "v71", "v72", "v73", "v74", "v75", "v76", "v77", "v78", "v79", "v80", "v81", "v82", "v83", "v84", "v96", "v97", "v98", "v99", "v100", "v101", "v102", "v103", "v104", "v105", "v106", "v107", "v108", "v109", "v110", "v111", "v112", "v113", "v114", "v115", "v116", "v117", "v118", "v119", "v120", "v121", "v122", "v123", "v124", "v125", "v126", "v127", "scc", "vcc", "memory");
    }
  }
  asm volatile("MK_CODE_TO:");
}
}

extern "C" void kernel_launch(void* const* d_in, const int* in_sizes, int n_in, void* d_out, int out_size, void* d_ws, size_t ws_size,
                              hipStream_t stream) {
  (void)hipFuncSetAttribute((const void*)mk::mega, hipFuncAttributeMaxDynamicSharedMemorySize, mk::LDS_BYTES);
  mk::mega<<<256, 512, mk::LDS_BYTES, stream>>>((const ush*)d_in[0], (const ush*)d_in[1], (const ush*)d_in[2], (const ush*)d_in[4], (const ush*)d_in[5],
                                                (const ush*)d_in[6], (const ush*)d_in[7], (ush*)d_out, (char*)d_ws);
}
```
